```python
import torch

def _softmax(x, axis=-1):
    return torch.softmax(x, dim=axis)
def _gelu(x):
    return torch.nn.functional.gelu(x, approximate="tanh")
def _silu(x):
    return torch.nn.functional.silu(x)
def _sigmoid(x):
    return torch.sigmoid(x)
def _layernorm(x, axis=-1, eps=1e-5):
    m = x.mean(dim=axis, keepdim=True)
    v = x.var(dim=axis, keepdim=True, unbiased=False)
    return (x - m) / torch.sqrt(v + eps)
def _scatter_add(tgt, idx, src, axis=0):
    return tgt.clone().index_add_(axis, idx, src)
def _segment_sum(src, seg, n):
    out = torch.zeros((n,) + src.shape[1:], dtype=src.dtype)
    return out.index_add_(0, seg, src)
def _pad(x, pw):
    flat = tuple(v for pair in reversed(pw) for v in pair)
    return torch.nn.functional.pad(x, flat)

kernel_name = 'parallel_sum__grouped_kv_mqa__dense_activation_squared_relu__prompt'

def setup_inputs():

    _w = (lambda a: (lambda t: t.to(torch.bfloat16) if t.is_floating_point() else t)(torch.as_tensor(a)))
    def _A_setup():
        B, S, D, H = 1, 2048, 576, 32
        Dh = D // H
        return {
            "x": _w((lambda *s: torch.randn(*s, dtype=torch.bfloat16) * 0.05)(B, S, D)),
            "wq": _w((lambda *s: torch.randn(*s, dtype=torch.bfloat16) * 0.2)(D, H * Dh)),
            "wk": _w((lambda *s: torch.randn(*s, dtype=torch.bfloat16) * 0.2)(D, Dh)),
            "wv": _w((lambda *s: torch.randn(*s, dtype=torch.bfloat16) * 0.1)(D, Dh)),
            "wo": _w((lambda *s: torch.randn(*s, dtype=torch.bfloat16) * 0.1)(H * Dh, D)),
        }
    _A_in = _A_setup()

    def _B_setup():
        B, S, D, F = 1, 2048, 576, 4608
        return {
            "x": _w((lambda *s: torch.randn(*s, dtype=torch.bfloat16) * 0.05)(B, S, D)),
            "w1": _w((lambda *s: torch.randn(*s, dtype=torch.bfloat16) * 0.05)(D, F)),
            "b1": _w((lambda *s: torch.randn(*s, dtype=torch.bfloat16) * 0.8)(F)),
            "w2": _w((lambda *s: torch.randn(*s, dtype=torch.bfloat16) * 0.05)(F, D)),
            "b2": _w((lambda *s: torch.randn(*s, dtype=torch.bfloat16) * 12.8)(D)),
        }
    _B_in = _B_setup()

    return {
        "A_x": _A_in["x"],
        "A_wq": _A_in["wq"],
        "A_wk": _A_in["wk"],
        "A_wv": _A_in["wv"],
        "A_wo": _A_in["wo"],
        "B_x": _B_in["x"],
        "B_w1": _B_in["w1"],
        "B_b1": _B_in["b1"],
        "B_w2": _B_in["w2"],
        "B_b2": _B_in["b2"],
    }

def reference(A_x, A_wq, A_wk, A_wv, A_wo, B_x, B_w1, B_b1, B_w2, B_b2):
    def _A_ref(x, wq, wk, wv, wo):
        B, S, D = x.shape
        H = 32
        Dh = D // H
        q = (lambda x, *d: x.permute(*d))((x @ wq).reshape(B, S, H, Dh), 0, 2, 1, 3)
        k = x @ wk
        v = x @ wv
        scores = torch.einsum("bhqd,bkd->bhqk", q, k) * (Dh ** -0.5)
        p = _softmax(scores, axis=-1)
        o = torch.einsum("bhqk,bkd->bhqd", p, v)
        return (lambda x, *d: x.permute(*d))(o, 0, 2, 1, 3).reshape(B, S, H * Dh) @ wo

    def _B_ref(x, w1, b1, w2, b2):
        h = torch.einsum("bsd,df->bsf", x, w1) + b1
        zero = torch.zeros_like(h)
        r = torch.where(h > zero, h, zero)
        h = r * r
        out = torch.einsum("bsf,fd->bsd", h, w2) + b2
        return out

    x = A_x
    xn = _layernorm(x, axis=-1)
    return x + _A_ref(xn, A_wq, A_wk, A_wv, A_wo) + _B_ref(xn, B_w1, B_b1, B_w2, B_b2)
```

```cpp
#include <hip/hip_runtime.h>
#include <hip/hip_bf16.h>
typedef __hip_bfloat16 bf16;
constexpr int S = 2048, D = 576, H = 32, DH = 18, F = 4608;

__global__ void ln_k(const bf16* x, bf16* xn) {
  int r = blockIdx.x; int t = threadIdx.x;
  float v[9]; float s = 0;
  for (int i = 0; i < 9; ++i) { v[i] = __bfloat162float(x[r * D + t + 64 * i]); s += v[i]; }
  for (int o = 32; o; o >>= 1) s += __shfl_xor(s, o);
  float m = s / D; float q = 0;
  for (int i = 0; i < 9; ++i) { float d = v[i] - m; q += d * d; }
  for (int o = 32; o; o >>= 1) q += __shfl_xor(q, o);
  float rs = rsqrtf(q / D + 1e-5f);
  for (int i = 0; i < 9; ++i) xn[r * D + t + 64 * i] = __float2bfloat16((v[i] - m) * rs);
}
template<int MODE>
__global__ void gemm_k(const bf16* A, const bf16* B, bf16* C, const bf16* bias, int M, int N, int K) {
  __shared__ float As[16][17], Bs[16][17];
  int tx = threadIdx.x, ty = threadIdx.y;
  int row = blockIdx.y * 16 + ty, col = blockIdx.x * 16 + tx;
  float acc = 0;
  for (int k0 = 0; k0 < K; k0 += 16) {
    As[ty][tx] = __bfloat162float(A[row * K + k0 + tx]);
    Bs[ty][tx] = col < N ? __bfloat162float(B[(k0 + ty) * N + col]) : 0.f;
    __syncthreads();
    for (int k = 0; k < 16; ++k) acc += As[ty][k] * Bs[k][tx];
    __syncthreads();
  }
  if (col < N) {
    if (MODE == 1) { acc += __bfloat162float(bias[col]); acc = acc > 0 ? acc * acc : 0.f; }
    C[row * N + col] = __float2bfloat16(acc);
  }
}
__global__ void attn_k(const bf16* q, const bf16* k, const bf16* v, bf16* o) {
  int idx = blockIdx.x * blockDim.x + threadIdx.x;
  int r = idx / H, h = idx % H;
  float qv[DH], acc[DH];
  for (int d = 0; d < DH; ++d) { qv[d] = __bfloat162float(q[r * D + h * DH + d]) * rsqrtf((float)DH); acc[d] = 0; }
  float m = -INFINITY, l = 0;
  for (int j = 0; j < S; ++j) {
    float s = 0;
    for (int d = 0; d < DH; ++d) s += qv[d] * __bfloat162float(k[j * DH + d]);
    float mn = fmaxf(m, s);
    float a = __expf(m - mn), p = __expf(s - mn);
    l = l * a + p;
    for (int d = 0; d < DH; ++d) acc[d] = acc[d] * a + p * __bfloat162float(v[j * DH + d]);
    m = mn;
  }
  for (int d = 0; d < DH; ++d) o[r * D + h * DH + d] = __float2bfloat16(acc[d] / l);
}
__global__ void final_k(const bf16* x, const bf16* hh, const bf16* o, const bf16* w2, const bf16* wo, const bf16* b2, bf16* out) {
  __shared__ float As[16][17], Bs[16][17];
  int tx = threadIdx.x, ty = threadIdx.y;
  int row = blockIdx.y * 16 + ty, col = blockIdx.x * 16 + tx;
  float acc = 0;
  for (int k0 = 0; k0 < F; k0 += 16) {
    As[ty][tx] = __bfloat162float(hh[row * F + k0 + tx]);
    Bs[ty][tx] = __bfloat162float(w2[(k0 + ty) * D + col]);
    __syncthreads();
    for (int k = 0; k < 16; ++k) acc += As[ty][k] * Bs[k][tx];
    __syncthreads();
  }
  for (int k0 = 0; k0 < D; k0 += 16) {
    As[ty][tx] = __bfloat162float(o[row * D + k0 + tx]);
    Bs[ty][tx] = __bfloat162float(wo[(k0 + ty) * D + col]);
    __syncthreads();
    for (int k = 0; k < 16; ++k) acc += As[ty][k] * Bs[k][tx];
    __syncthreads();
  }
  out[row * D + col] = __float2bfloat16(acc + __bfloat162float(b2[col]) + __bfloat162float(x[row * D + col]));
}

extern "C" void kernel_launch(void* const* d_in, const int* in_sizes, int n_in,
                              void* d_out, int out_size, void* d_ws, size_t ws_size,
                              hipStream_t stream) {
  const bf16* x = (const bf16*)d_in[0];
  const bf16* wq = (const bf16*)d_in[1];
  const bf16* wk = (const bf16*)d_in[2];
  const bf16* wv = (const bf16*)d_in[3];
  const bf16* wo = (const bf16*)d_in[4];
  const bf16* w1 = (const bf16*)d_in[6];
  const bf16* b1 = (const bf16*)d_in[7];
  const bf16* w2 = (const bf16*)d_in[8];
  const bf16* b2 = (const bf16*)d_in[9];
  bf16* out = (bf16*)d_out;
  bf16* ws = (bf16*)d_ws;
  bf16* xn = ws; bf16* q = xn + S * D; bf16* k = q + S * D; bf16* v = k + S * 32; bf16* o = v + S * 32; bf16* hh = o + S * D;
  ln_k<<<S, 64, 0, stream>>>(x, xn);
  gemm_k<0><<<dim3(D / 16, S / 16), dim3(16, 16), 0, stream>>>(xn, wq, q, nullptr, S, D, D);
  gemm_k<0><<<dim3(2, S / 16), dim3(16, 16), 0, stream>>>(xn, wk, k, nullptr, S, DH, D);
  gemm_k<0><<<dim3(2, S / 16), dim3(16, 16), 0, stream>>>(xn, wv, v, nullptr, S, DH, D);
  gemm_k<1><<<dim3(F / 16, S / 16), dim3(16, 16), 0, stream>>>(xn, w1, hh, b1, S, F, D);
  attn_k<<<S * H / 256, 256, 0, stream>>>(q, k, v, o);
  final_k<<<dim3(D / 16, S / 16), dim3(16, 16), 0, stream>>>(x, hh, o, w2, wo, b2, out);
}
```

```cpp
#define G1PRIO 2
#define G2PRIO 0
#include <hip/hip_runtime.h>
#include <hip/hip_bf16.h>
#include <stdint.h>
#include <type_traits>

typedef unsigned short u16;
using bf16x8 = __attribute__((ext_vector_type(8))) short;
using s16x4 = __attribute__((ext_vector_type(4))) short;
using f32x16 = __attribute__((ext_vector_type(16))) float;
using f32x4 = __attribute__((ext_vector_type(4))) float;
using u32x4 = __attribute__((ext_vector_type(4))) unsigned;
using u32x2 = __attribute__((ext_vector_type(2))) unsigned;
typedef float f32x2_t __attribute__((ext_vector_type(2)));
typedef __bf16 bf16x2_t __attribute__((ext_vector_type(2)));
#define LAS __attribute__((address_space(3)))
#define GAS __attribute__((address_space(1)))
typedef LAS char* lds_ptr;
typedef GAS unsigned gu32;
typedef short v4i16_t __attribute__((ext_vector_type(4)));

#define DI __device__ __forceinline__
#define SBAR() __builtin_amdgcn_sched_barrier(0)
#define PIN(x) asm volatile("" : "+v"(x))
#define PINS(x) asm volatile("" : "+s"(x))
#define MFMA(a, b, c) __builtin_amdgcn_mfma_f32_32x32x16_bf16((a), (b), (c), 0, 0, 0)
#define BC(x) __builtin_bit_cast(bf16x8, x)
#define RLX_AGENT __ATOMIC_RELAXED, __HIP_MEMORY_SCOPE_AGENT

constexpr int S = 2048, D = 576, H = 32, DH = 18, F = 4608;
constexpr float C2 = 0.23570226039551584f * 1.4426950408889634f;

constexpr unsigned WS_FLAGS = 0;
constexpr unsigned FL_H = 0, FL_O = 256 * 128, FL_P = 512 * 128, FL_BYTES = 768 * 128;
constexpr unsigned WS_XNF = 131072;
constexpr unsigned WS_KV = WS_XNF + 64 * 36 * 1024;
constexpr unsigned WS_OF = WS_KV + 196608;
constexpr unsigned WS_OX = WS_OF + 64 * 32 * 1024;
constexpr unsigned WS_HF = WS_OX + 64 * 16 * 256;
constexpr unsigned WS_PART = WS_HF + 64 * 288 * 1024;
constexpr unsigned WS_END = WS_PART + 64 * 16 * 18 * 1024;

constexpr int REC = 2560, CHUNK = 8 * REC;
constexpr int GSLOT = 36864;
constexpr int L_EX = 0, L_BIAS = 5120, L_SYNC = L_BIAS + 1152, L_G = 6400, L_KV = L_G + 3 * GSLOT, L_TOTAL = L_KV + 2 * CHUNK;
constexpr int L_Q = L_KV + CHUNK, QROW = 88;
constexpr int L_Q1 = L_Q + 128 * QROW;
constexpr int SY_R = 0, SY_A = 4, SY_Q = 8  , SY_F = 24, SY_L = 28;

DI unsigned cvtpk(float lo, float hi) { f32x2_t v = {lo, hi}; bf16x2_t b = __builtin_convertvector(v, bf16x2_t); return __builtin_bit_cast(unsigned, b); }
DI float bf_lo(unsigned u) { return __uint_as_float(u << 16); }
DI float bf_hi(unsigned u) { return __uint_as_float(u & 0xffff0000u); }
DI float bfr(float x) { return bf_lo(cvtpk(x, x) & 0xffffu); }
DI s16x4 trread(lds_ptr p) { return __builtin_bit_cast(s16x4, __builtin_amdgcn_ds_read_tr16_b64_v4i16((LAS v4i16_t*)p)); }
DI bf16x8 cat8(s16x4 a, s16x4 b) { return __builtin_shufflevector(a, b, 0, 1, 2, 3, 4, 5, 6, 7); }
template <int LI, int GI>
DI void glds16(unsigned voff, const void* sbase, unsigned ldsw) {
  asm volatile("s_add_u32 m0, %2, %3\n\ts_nop 0\n\tglobal_load_lds_dwordx4 %0, %1 offset:%4"
               :: "v"(voff), "s"(sbase), "s"(ldsw), "i"(LI - GI), "i"(GI) : "memory", "scc");
}
DI u32x4 mksrd(const void* p, unsigned bytes) { const unsigned long long a = (unsigned long long)p; return u32x4{(unsigned)a, (unsigned)(a >> 32) & 0xffffu, bytes, 0x00020000u}; }
template <int LI>
DI void bdma16(unsigned voff, u32x4 srd, unsigned soff, unsigned ldsw) {
  asm volatile("s_add_u32 m0, %3, %4\n\ts_nop 0\n\tbuffer_load_dwordx4 %0, %1, %2 offen lds"
               :: "v"(voff), "s"(srd), "s"(soff), "s"(ldsw), "i"(LI) : "memory", "scc");
}
template <int LI>
DI void bdma16r(unsigned voff, u32x4 srd, unsigned& soff, unsigned inc, unsigned ldsw) {
  asm volatile("s_add_u32 m0, %3, %4\n\ts_add_u32 %0, %0, %5\n\tbuffer_load_dwordx4 %1, %2, %0 offen lds"
               : "+s"(soff) : "v"(voff), "s"(srd), "s"(ldsw), "i"(LI), "s"(inc) : "memory", "scc");
}
template <int OFF>
DI void bload16_sc1(u32x4& dst, unsigned voff, u32x4 srd, unsigned soff) {
  asm volatile("buffer_load_dwordx4 %0, %1, %2, %3 offen offset:%4 sc1" : "=v"(dst) : "v"(voff), "s"(srd), "s"(soff), "i"(OFF) : "memory");
}
template <int OFF>
DI void gload16(bf16x8& dst, unsigned voff, const void* sbase) { asm volatile("global_load_dwordx4 %0, %1, %2 offset:%3" : "=v"(dst) : "v"(voff), "s"(sbase), "i"(OFF) : "memory"); }
template <int N> DI void waitv() { asm volatile("s_waitcnt vmcnt(%c0)" :: "i"(N) : "memory"); }
DI void arrive(unsigned addr, unsigned one) { asm volatile("s_mov_b64 exec, 1\n\tds_add_u32 %0, %1\n\ts_mov_b64 exec, -1" :: "v"(addr), "v"(one) : "memory"); }
DI void check(unsigned v, unsigned addr, unsigned target) {
  unsigned s;
  asm volatile(
      "s_nop 0\n\t"
      "v_readfirstlane_b32 %1, %0\n\t"
      "s_cmp_ge_u32 %1, %3\n\t"
      "s_cbranch_scc1 2f\n\t"
      "1:\n\t"
      "ds_read_b32 %0, %2\n\t"
      "s_waitcnt lgkmcnt(0)\n\t"
      "v_readfirstlane_b32 %1, %0\n\t"
      "s_cmp_ge_u32 %1, %3\n\t"
      "s_cbranch_scc0 1b\n\t"
      "2:"
      : "+v"(v), "=&s"(s) : "v"(addr), "s"(target) : "memory", "scc");
}
DI void gload16_sc1(u32x4& dst, unsigned voff, const void* sbase) { asm volatile("global_load_dwordx4 %0, %1, %2 sc1" : "=v"(dst) : "v"(voff), "s"(sbase) : "memory"); }
DI void gload8_sc1(u32x2& dst, unsigned voff, const void* sbase) { asm volatile("global_load_dwordx2 %0, %1, %2 sc1" : "=v"(dst) : "v"(voff), "s"(sbase) : "memory"); }
#define WAITV(N) asm volatile("s_waitcnt vmcnt(" #N ")" ::: "memory")

#define TS(kid, i) do {} while (0)
#define HWID(kid) do {} while (0)

template <int VM>
DI void swbar(unsigned addr, unsigned target) {
  unsigned v, s;
  asm volatile(
      "s_waitcnt vmcnt(%c4) lgkmcnt(0)\n\t"
      "s_mov_b64 exec, 1\n\t"
      "v_mov_b32 %0, 1\n\t"
      "ds_add_rtn_u32 %0, %2, %0\n\t"
      "s_waitcnt lgkmcnt(0)\n\t"
      "v_add_u32 %0, 1, %0\n\t"
      "1:\n\t"
      "s_nop 0\n\t"
      "v_readfirstlane_b32 %1, %0\n\t"
      "s_nop 1\n\t"
      "s_cmp_ge_u32 %1, %3\n\t"
      "s_cbranch_scc1 2f\n\t"
      "ds_read_b32 %0, %2\n\t"
      "s_waitcnt lgkmcnt(0)\n\t"
      "s_branch 1b\n\t"
      "2:\n\t"
      "s_mov_b64 exec, -1"
      : "=&v"(v), "=&s"(s) : "v"(addr), "s"(target), "i"(VM) : "memory", "scc");
}
DI void lds_wait(unsigned addr) {
  unsigned v, s;
  asm volatile(
      "1:\n\t"
      "ds_read_b32 %0, %2\n\t"
      "s_waitcnt lgkmcnt(0)\n\t"
      "v_readfirstlane_b32 %1, %0\n\t"
      "s_nop 1\n\t"
      "s_cmp_lg_u32 %1, 0\n\t"
      "s_cbranch_scc1 2f\n\t"
      "s_sleep 2\n\t"
      "s_branch 1b\n\t"
      "2:"
      : "=&v"(v), "=&s"(s) : "v"(addr) : "memory", "scc");
}
DI void lds_set(unsigned addr) {
  unsigned v;
  asm volatile("s_waitcnt lgkmcnt(0)\n\tv_mov_b32 %0, 1\n\tds_write_b32 %1, %0" : "=&v"(v) : "v"(addr) : "memory");
}
template <int SLEEP>
DI void gpoll(gu32* p, unsigned target) {
  for (unsigned i = 0; i < (1u << 18); ++i) {
    if ((unsigned)__builtin_amdgcn_readfirstlane(__hip_atomic_load(p, RLX_AGENT)) >= target) break;
    __builtin_amdgcn_s_sleep(SLEEP);
  }
  __builtin_amdgcn_fence(__ATOMIC_ACQUIRE, "wavefront");
  asm volatile("" ::: "memory");
}
DI void gsignal(gu32* p, int lane) { if (lane == 0) (void)__hip_atomic_fetch_add(p, 1u, RLX_AGENT); }

__global__ __launch_bounds__(768) void k_ln_kv(const u16* __restrict__ x, const u16* __restrict__ wk, const u16* __restrict__ wv, char* __restrict__ ws) {
  __shared__ __attribute__((aligned(16))) float red[12 * 26 * 64];
  __shared__ __attribute__((aligned(16))) float stats[32 * 52];
  const int mt = blockIdx.x, tid = threadIdx.x, lane = tid & 63, m = lane & 31, h = lane >> 5;
  const int w = __builtin_amdgcn_readfirstlane(tid >> 6);
  if (tid < 256) { TS(0, 0); HWID(0); }
  u32x4 xr[3];
#pragma unroll
  for (int i = 0; i < 3; ++i) xr[i] = *(const u32x4*)(x + (size_t)(32 * mt + m) * D + 16 * (3 * w + i) + 8 * h);
  const int c0 = lane, c1 = lane + 64 < 108 ? lane + 64 : 107;
  const u32x4 k0 = *(const u32x4*)((const char*)wk + w * 1728 + c0 * 16), k1 = *(const u32x4*)((const char*)wk + w * 1728 + c1 * 16);
  const u32x4 v0 = *(const u32x4*)((const char*)wv + w * 1728 + c0 * 16), v1 = *(const u32x4*)((const char*)wv + w * 1728 + c1 * 16);
  if (tid < 12) *(unsigned*)(ws + WS_FLAGS + (mt * 12 + tid) * 128) = 0u;
  char* img = (char*)red + w * 6656;
#pragma unroll
  for (int j = 0; j < 4; ++j) {
    const int d0 = c0 * 4 + j, r0 = (d0 * 57) >> 9, d1 = c1 * 4 + j, r1 = (d1 * 57) >> 9;
    *(unsigned*)(img + r0 * 64 + (d0 - 9 * r0) * 4) = k0[j];
    *(unsigned*)(img + r1 * 64 + (d1 - 9 * r1) * 4) = k1[j];
    *(unsigned*)(img + 3072 + r0 * 64 + (d0 - 9 * r0) * 4) = v0[j];
    *(unsigned*)(img + 3072 + r1 * 64 + (d1 - 9 * r1) * 4) = v1[j];
  }
  float v[24];
#pragma unroll
  for (int i = 0; i < 3; ++i)
#pragma unroll
    for (int j = 0; j < 4; ++j) { v[8 * i + 2 * j] = bf_lo(xr[i][j]); v[8 * i + 2 * j + 1] = bf_hi(xr[i][j]); }
  float s = 0.f;
#pragma unroll
  for (int i = 0; i < 24; ++i) s += v[i];
  const float lm = s * (1.f / 24.f);
  float m2 = 0.f;
#pragma unroll
  for (int i = 0; i < 24; ++i) { const float d = v[i] - lm; m2 = __builtin_fmaf(d, d, m2); }
  *(f32x2_t*)&stats[m * 52 + (2 * w + h) * 2] = f32x2_t{lm, m2};
  const int g4 = lane >> 4, i16 = lane & 15;
  const lds_ptr rd = (lds_ptr)img + ((g4 >> 1) * 8 + (i16 >> 2)) * 64 + (g4 & 1) * 32 + (i16 & 3) * 8;
  bf16x8 kf[3], vf[3];
#pragma unroll
  for (int i = 0; i < 3; ++i) {
    kf[i] = cat8(trread(rd + i * 1024), trread(rd + i * 1024 + 256));
    vf[i] = cat8(trread(rd + 3072 + i * 1024), trread(rd + 3072 + i * 1024 + 256));
  }
  if (tid < 256) TS(0, 1);
  __syncthreads();
  if (tid < 256) TS(0, 2);
  float pm[24], mean = 0.f, M2 = 0.f;
#pragma unroll
  for (int i = 0; i < 12; ++i) { const f32x4 t = *(const f32x4*)&stats[m * 52 + 4 * i]; pm[2 * i] = t[0]; pm[2 * i + 1] = t[2]; mean += t[0] + t[2]; M2 += t[1] + t[3]; }
  mean *= (1.f / 24.f);
  float dd = 0.f;
#pragma unroll
  for (int i = 0; i < 24; ++i) { const float d = pm[i] - mean; dd = __builtin_fmaf(d, d, dd); }
  const float var = (M2 + 24.f * dd) * (1.f / 576.f);
  const float mb = bfr(mean), sd = bfr(__builtin_amdgcn_sqrtf(bfr(bfr(var) + 1e-5f)));
  const float rinv = __builtin_amdgcn_rcpf(sd);
  f32x16 accK = {}, accV = {};
#pragma unroll
  for (int i = 0; i < 3; ++i) {
    u32x4 o;
#pragma unroll
    for (int j = 0; j < 4; ++j) {
      const unsigned dp = cvtpk(v[8 * i + 2 * j] - mb, v[8 * i + 2 * j + 1] - mb);
      o[j] = cvtpk(bf_lo(dp) * rinv, bf_hi(dp) * rinv);
    }
    *(u32x4*)(ws + WS_XNF + ((size_t)mt * 36 + 3 * w + i) * 1024 + lane * 16) = o;
    accK = MFMA(kf[i], BC(o), accK);
    accV = MFMA(BC(o), vf[i], accV);
  }
#pragma unroll
  for (int r = 0; r < 10; ++r) red[(w * 26 + r) * 64 + lane] = accK[r];
#pragma unroll
  for (int r = 0; r < 16; ++r) red[(w * 26 + 10 + r) * 64 + lane] = accV[r];
  __syncthreads();
  if (tid < 256) TS(0, 3);
  for (int idx = w; idx < 13; idx += 12) {
    float a = 0.f, b = 0.f;
#pragma unroll
    for (int ww = 0; ww < 12; ++ww) { a += red[(ww * 26 + 2 * idx) * 64 + lane]; b += red[(ww * 26 + 2 * idx + 1) * 64 + lane]; }
    unsigned p = cvtpk(a, b);
    if (idx < 4) *(unsigned*)(ws + WS_KV + (size_t)mt * REC + lane * 16 + idx * 4) = p;
    else if (idx == 4) { if (h == 0) *(unsigned*)(ws + WS_KV + (size_t)mt * REC + 1024 + m * 4) = p; }
    else {
      const int dv = idx - 5;
      if (m == DH) p = 0x3f803f80u;
      if (m <= DH) *(unsigned*)(ws + WS_KV + (size_t)(mt + 1) * REC + 1152 + ((dv >> 2) * 2 + h) * 304 + m * 16 + (dv & 3) * 4) = p;
    }
  }
  if (tid < 256) TS(0, 4);
}

constexpr float L_TRIG = 1073741824.f;
constexpr float L_BAD = 1.2980742e33f;
constexpr int M_OFF = 40;

DI void set_negm(u32x4& q1, float m, int h) {
  const float nm = -m;
  const unsigned hi = __float_as_uint(nm) & 0xffff0000u;
  const float r1 = nm - __uint_as_float(hi);
  const unsigned mid = __float_as_uint(r1) & 0xffff0000u;
  const float r2 = r1 - __uint_as_float(mid);
  q1[1] = h ? 0u : ((hi >> 16) | mid);
  q1[2] = h ? 0u : (__float_as_uint(r2) >> 16);
}
DI float half_max(float v) {
  auto rr = __builtin_amdgcn_permlane32_swap(__float_as_uint(v), __float_as_uint(v), false, false);
  return __builtin_fmaxf(__uint_as_float(rr[0]), __uint_as_float(rr[1]));
}
DI float tile_max(const f32x16& s) {
  float a = __builtin_fmaxf(s[0], s[1]);
#pragma unroll
  for (int r = 2; r < 16; ++r) a = __builtin_fmaxf(a, s[r]);
  return half_max(a);
}
DI f32x16 attn_slow(const char* kv, bf16x8 q0, u32x4 q1, int lane) {
  const int m = lane & 31, h = lane >> 5, vr = m < DH ? m : DH;
  q1[1] = 0u; q1[2] = 0u;
  float mx = -__builtin_inff();
  f32x16 o = f32x16{};
#pragma unroll 1
  for (int t = 0; t < 64; ++t) {
    const char* rk = kv + (size_t)t * REC; const char* rv = rk + REC + 1152 + h * 304 + vr * 16;
    const bf16x8 k0 = *(const bf16x8*)(rk + lane * 16);
    u32x4 k1 = {*(const unsigned*)(rk + 1024 + m * 4), 0u, 0u, 0u};
    const bf16x8 v0 = *(const bf16x8*)rv, v1 = *(const bf16x8*)(rv + 608);
    f32x16 s = MFMA(BC(k1), BC(q1), f32x16{});
    s = MFMA(k0, q0, s);
    const float mn = __builtin_fmaxf(mx, tile_max(s));
    const float al = __builtin_amdgcn_exp2f(mx - mn);
    mx = mn;
    u32x4 p0, p1;
#pragma unroll
    for (int j = 0; j < 4; ++j) {
      p0[j] = cvtpk(__builtin_amdgcn_exp2f(s[2 * j] - mn), __builtin_amdgcn_exp2f(s[2 * j + 1] - mn));
      p1[j] = cvtpk(__builtin_amdgcn_exp2f(s[8 + 2 * j] - mn), __builtin_amdgcn_exp2f(s[8 + 2 * j + 1] - mn));
    }
#pragma unroll
    for (int r = 0; r < 16; ++r) o[r] *= al;
    o = MFMA(v0, BC(p0), o);
    o = MFMA(v1, BC(p1), o);
  }
  return o;
}
DI unsigned relu2pk(float a, float b) {
  unsigned r; float t;
  asm volatile("v_mul_f32_e64 %0, %2, |%2|\n\tv_mul_f32_e64 %1, %3, |%3|\n\tv_cvt_pk_bf16_f32 %0, %0, %1\n\tv_pk_max_i16 %0, %0, 0" : "=&v"(r), "=&v"(t) : "v"(a), "v"(b));
  return r;
}
DI void mfma_dc(f32x16& d, bf16x8 a, bf16x8 b, const f32x16& c) { asm volatile("v_mfma_f32_32x32x16_bf16 %0, %1, %2, %3" : "=&v"(d) : "v"(a), "v"(b), "v"(c)); }
DI float mul_abs(float a) { float r; asm("v_mul_f32_e64 %0, %1, |%1|" : "=v"(r) : "v"(a)); return r; }
DI unsigned pk_relu(unsigned p) { unsigned r; asm("v_pk_max_i16 %0, %1, 0" : "=v"(r) : "v"(p)); return r; }

template <int N, class Fn> DI void static_for(Fn&& f) {
  if constexpr (N > 0) { static_for<N - 1>(f); f(std::integral_constant<int, N - 1>{}); }
}

__global__ __launch_bounds__(512, 2) void k_main(const u16* __restrict__ x, const u16* __restrict__ wq, const u16* __restrict__ wo, const u16* __restrict__ w1,
                                                 const u16* __restrict__ b1, const u16* __restrict__ w2, const u16* __restrict__ b2, char* __restrict__ ws, u16* __restrict__ out) {
  extern __shared__ __attribute__((aligned(16))) char smem[];
  const int xcd = blockIdx.x & 7, bidx = blockIdx.x >> 3;
  const int kq = xcd & 3, nq = bidx & 3, cg = kq * 4 + nq, rb = (xcd >> 2) * 8 + (bidx >> 2);
  const int tid = threadIdx.x, lane = tid & 63, m = lane & 31, h = lane >> 5;
  const int w8 = __builtin_amdgcn_readfirstlane(tid >> 6), w = w8 & 3;
  const int mt = rb * 4 + w;
  const unsigned lds0 = (unsigned)(uintptr_t)(lds_ptr)smem;
  const unsigned voff = lane * 16;
  const int n0q = (36 * cg) & ~7;
#define MKRS(off, bytes) __builtin_amdgcn_make_buffer_rsrc((void*)(ws + (off)), 0, (int)(bytes), 0x00020000)
  gu32* flags = (gu32*)(ws + WS_FLAGS);

  if (w8 < 4) {
    TS(1, 0); HWID(1);
    const int prow = lane >> 2, pch = lane & 3;
    const unsigned ldsw = lds0 + L_G + 9 * w * 1024;
    u32x4 srdW = mksrd(wq, D * D * 2);
    {
      const unsigned vq0 = (unsigned)((prow * D + n0q + pch * 8) * 2), vq1 = (unsigned)((lane * D + n0q + 32) * 2);
      static_for<9>([&](auto p) { bdma16<p * 1024>(vq0, srdW, (unsigned)((9 * w + p) * 16 * D * 2), ldsw); });
      bdma16<L_Q1>(vq1, srdW, (unsigned)(w * 64 * D * 2), lds0 + w * 1024);
      bdma16<L_Q1 + 4096>(vq1, srdW, (unsigned)((w + 4) * 64 * D * 2), lds0 + w * 1024);
      bdma16<L_Q1 + 8192>(vq1, srdW, (unsigned)(8 * 64 * D * 2), lds0);
    }
    bf16x8 xf[36];
    {
      const char* xb = ws + WS_XNF + (size_t)mt * 36 * 1024;
      static_for<36>([&](auto ks) { gload16<(ks & 3) * 1024>(xf[ks], voff, xb + (ks >> 2) * 4096); });
    }
    srdW = mksrd(w1, D * F * 2);
    unsigned sB = 16 * F * 2, sB4 = sB, sB8 = sB, so0 = (unsigned)(9 * w * 16 * F * 2) - sB, so = so0;
    unsigned voffA = (unsigned)((prow * F + pch * 8 + 288 * cg) * 2), voffB;
    unsigned aR = lds0 + L_SYNC + SY_R, aL = lds0 + L_SYNC + SY_L, one = 1u;
    PIN(aR); PIN(aL); PIN(one);
    const int g4 = lane >> 4, i16 = lane & 15;
    const lds_ptr rd0 = (lds_ptr)smem + L_G + ((g4 >> 1) * 8 + (i16 >> 2)) * 64 + (g4 & 1) * 32 + (i16 & 3) * 8;
    lds_ptr biasp = (lds_ptr)smem + L_BIAS + h * 16;
    asm volatile("s_waitcnt vmcnt(36) lgkmcnt(0)\n\ts_barrier" ::: "memory");
    TS(1, 1);
    __builtin_amdgcn_s_setprio(G1PRIO);
    constexpr int PD = 4;
    s16x4 wlo[5], whi[5];
    f32x16 A, P, Bi;
    {
      const lds_ptr rq1 = (lds_ptr)smem + L_Q1 + ((g4 >> 1) * 8 + (i16 >> 2)) * 16 + (i16 & 1) * 8;
      s16x4 ulo[2], uhi[2];
#pragma unroll
      for (int i = 0; i < 2; ++i) { wlo[i] = trread(rd0 + i * 1024); whi[i] = trread(rd0 + i * 1024 + 256); ulo[i] = trread(rq1 + i * 256); uhi[i] = trread(rq1 + i * 256 + 64); }
      static_for<36>([&](auto ksc) {
        constexpr int ks = ksc;
        if constexpr ((ks & 1) == 0) { waitv<34 - ks + ks / 2>(); PIN(xf[ks]); PIN(xf[ks + 1]); }
        const bf16x8 wf = cat8(wlo[ks & 1], whi[ks & 1]), uf = cat8(ulo[ks & 1], uhi[ks & 1]);
        if constexpr (ks == 0) A = MFMA(wf, xf[ks], f32x16{}); else A = MFMA(wf, xf[ks], A);
        PIN(A); SBAR();
        if constexpr (ks + 2 < 36) { wlo[ks & 1] = trread(rd0 + (ks + 2) * 1024); whi[ks & 1] = trread(rd0 + (ks + 2) * 1024 + 256); }
        SBAR();
        if constexpr (ks == 0) P = MFMA(uf, xf[ks], f32x16{}); else P = MFMA(uf, xf[ks], P);
        PIN(P); SBAR();
        if constexpr (ks + 2 < 36) { ulo[ks & 1] = trread(rq1 + (ks + 2) * 256); uhi[ks & 1] = trread(rq1 + (ks + 2) * 256 + 64); }
        if constexpr (ks == 33) arrive(aR, one);
        if constexpr (ks & 1) {
          constexpr int q = ks >> 1;
          if constexpr (q == 9) { voffA += 64; so = so0; }
          bdma16r<(1 + q / 9) * GSLOT + (q % 9) * 1024>(voffA, srdW, so, sB, ldsw);
        }
        SBAR();
      });
      waitv<9>();
      arrive(aL, one);
      const lds_ptr qw = (lds_ptr)smem + L_Q + (32 * w + m) * QROW + h * 8;
#pragma unroll
      for (int g = 0; g < 4; ++g) *(LAS u32x2*)(qw + g * 16) = u32x2{cvtpk(A[4 * g] * C2, A[4 * g + 1] * C2), cvtpk(A[4 * g + 2] * C2, A[4 * g + 3] * C2)};
      *(LAS u32x2*)(qw + 64) = u32x2{cvtpk(P[0] * C2, P[1] * C2), cvtpk(P[2] * C2, P[3] * C2)};
      lds_set(lds0 + L_SYNC + SY_Q + w * 4);
      check(0u, aL, 4u);
    }
    TS(1, 2);
    const unsigned vA2 = (unsigned)((((lane >> 2) & 3) * D + (lane >> 4) * 32 + (lane & 3) * 8 + 144 * nq) * 2);
    const unsigned vB2 = (unsigned)(((lane >> 1) * D + 128 + (lane & 1) * 8 + 144 * nq) * 2);
    auto set_g2 = [&](int j) {
      int stp = 18 * nq + 8 * j + 2 * w; stp = stp >= 72 ? stp - 72 : stp;
      const unsigned r = (unsigned)((1152 * kq + 16 * stp) * D * 2);
      voffA = vA2 + r; voffB = vB2 + r;
    };
    auto bias_read = [&]() {
#pragma unroll
      for (int g = 0; g < 4; ++g) { const f32x4 bv = *(const LAS f32x4*)(biasp + g * 32); Bi[4 * g] = bv[0]; Bi[4 * g + 1] = bv[1]; Bi[4 * g + 2] = bv[2]; Bi[4 * g + 3] = bv[3]; }
      biasp += 128;
    };
    const __amdgpu_buffer_rsrc_t rsh = MKRS(WS_HF + (unsigned)(mt * 288 + 18 * cg) * 1024, 18 * 1024);
    unsigned hst = voff + 16384u;
    unsigned ep[8];
    int sl_cur = GSLOT, sl_dma = 0;
    lds_ptr rd = rd0 + GSLOT;
    bias_read();
#pragma unroll
    for (int i = 0; i < PD; ++i) { wlo[i] = trread(rd + i * 1024); whi[i] = trread(rd + i * 1024 + 256); }
#pragma unroll 1
    for (int t = 0; t < 9; ++t) {
      if (t < 7) { voffA += 64; voffB = voffA; }
      else {
        if (t == 7) {
          srdW = mksrd(w2, F * D * 2);
          sB = 4 * D * 2; sB4 = sB; sB8 = 0u - 7u * sB; so0 = 0u - sB;
        }
        set_g2(t - 7);
      }
      const unsigned ldsd = ldsw + sl_dma;
      so = so0;
      const int sl_nxt = sl_cur == 2 * GSLOT ? 0 : sl_cur + GSLOT;
      unsigned cv;
      static_for<36>([&](auto ksc) {
        constexpr int ks = ksc;
        const bf16x8 wf = cat8(wlo[ks % PD], whi[ks % PD]);
        if constexpr (ks == 0) mfma_dc(A, wf, xf[ks], Bi); else if constexpr (ks == 35) mfma_dc(P, wf, xf[ks], A); else A = MFMA(wf, xf[ks], A);
        SBAR();
        if constexpr (ks == 4) check(cv, aR, 4u * (t + 1));
        if constexpr (ks == 36 - PD) { check(cv, aL, 4u * (t + 2)); rd = rd0 + sl_nxt; TS(1, 3 + t); }
        wlo[ks % PD] = trread(rd + ((ks + PD) % 36) * 1024); whi[ks % PD] = trread(rd + ((ks + PD) % 36) * 1024 + 256);
        if constexpr (ks == 0) cv = *(volatile LAS unsigned*)((lds_ptr)smem + L_SYNC + SY_R);
        if constexpr (ks == 28) cv = *(volatile LAS unsigned*)((lds_ptr)smem + L_SYNC + SY_L);
        if constexpr (ks == 35 - PD) arrive(aR, one);
        if constexpr (ks >= 4 && ks < 22 && (ks & 1) == 0) { constexpr int p = (ks - 4) >> 1; bdma16r<p * 1024>(p < 8 ? voffA : voffB, srdW, so, p == 4 ? sB4 : (p == 8 ? sB8 : sB), ldsd); }
        if constexpr (ks >= 3 && ks < 19 && (ks & 1) == 1) { constexpr int j = (ks - 3) >> 1; ep[j] = relu2pk(P[2 * j], P[2 * j + 1]); }
        if constexpr (ks == 21 || ks == 22) {
          constexpr int hh = ks - 21;
          __builtin_amdgcn_raw_buffer_store_b128(u32x4{ep[4 * hh], ep[4 * hh + 1], ep[4 * hh + 2], ep[4 * hh + 3]}, rsh, hst + hh * 1024, 0, 16);
        }
        if constexpr (ks == 23) { waitv<11>(); arrive(aL, one); hst = voff + (unsigned)t * 2048u; PIN(hst); }
        if constexpr (ks == 25) bias_read();
        SBAR();
      });
      sl_dma = sl_cur; sl_cur = sl_nxt;
    }
    u32x4 hf[8];
    u32x4 srdH = mksrd(ws + WS_HF + ((size_t)mt * 288 + 72 * kq) * 1024, 72 * 1024);
    unsigned sh[4];
    auto set_h = [&](int j) {
#pragma unroll
      for (int i = 0; i < 4; ++i) { int stp = 18 * nq + 8 * j + 2 * i; stp = stp >= 72 ? stp - 72 : stp; sh[i] = (unsigned)stp * 1024u; }
    };
    set_h(0);
    static_for<8>([&](auto i) { bload16_sc1<(i & 1) * 1024>(hf[i], voff, srdH, sh[i >> 1]); });
    const lds_ptr r2d0 = (lds_ptr)smem + L_G + (g4 >> 1) * 1024 + (i16 >> 2) * 64 + (g4 & 1) * 32 + (i16 & 3) * 8;
    const lds_ptr r2h0 = (lds_ptr)smem + L_G + 8192 + ((g4 >> 1) * 4 + (i16 >> 2)) * 32 + (i16 & 3) * 8;
    lds_ptr r2d = r2d0 + sl_cur, r2h = r2h0 + sl_cur;
#pragma unroll
    for (int tl = 0; tl < 4; ++tl) { wlo[tl] = trread(r2d + tl * 256); whi[tl] = trread(r2d + 2048 + tl * 256); }
    wlo[4] = trread(r2h); whi[4] = trread(r2h + 256);
    asm volatile("s_nop 11" : "+v"(P));
#pragma unroll
    for (int j = 0; j < 8; ++j) ep[j] = relu2pk(P[2 * j], P[2 * j + 1]);
    __builtin_amdgcn_raw_buffer_store_b128(u32x4{ep[0], ep[1], ep[2], ep[3]}, rsh, hst, 0, 16);
    __builtin_amdgcn_raw_buffer_store_b128(u32x4{ep[4], ep[5], ep[6], ep[7]}, rsh, hst + 1024, 0, 16);
    f32x16 acc[5] = {};
    waitv<2>();
    TS(1, 13);
    __builtin_amdgcn_s_setprio(G2PRIO);
    u32x2 ox0 = {0u, 0u}, ox1 = {0u, 0u};
#pragma unroll 1
    for (int j = 0; j < 10; ++j) {
      int nD, nH;
      const int sl_nxt = sl_cur == 2 * GSLOT ? 0 : sl_cur + GSLOT;
      nD = sl_nxt; nH = sl_nxt;
      if (j < 7) {
        set_g2(j + 2); set_h(j + 1);
        if (j == 1) { gsignal(flags + (FL_H + (mt * 4 + kq) * 128) / 4, lane); gpoll<1>(flags + (FL_H + (mt * 4 + kq) * 128) / 4, 4); TS(1, 24); }
      } else if (j == 7) {
        set_h(8);
        srdW = mksrd(wo, D * D * 2);
        sB4 = 6 * D * 2; sB8 = 0u - 30u * D * 2;
        const unsigned r = (unsigned)((144 * kq + 36 * w) * D * 2);
        voffA = vA2 + r; voffB = vB2 + r + (unsigned)(h * 2 * D * 2);
      } else if (j == 8) {
        srdW[2] = 0u;
        gpoll<4>(flags + (FL_O + (mt * 4 + kq) * 128) / 4, 4); TS(1, 25);
        srdH = mksrd(ws + WS_OF + ((size_t)mt * 32 + 8 * kq) * 1024, 8 * 1024);
#pragma unroll
        for (int i = 0; i < 4; ++i) sh[i] = (unsigned)i * 2048u;
        const char* oxb = ws + WS_OX + (size_t)(mt * 16 + 4 * kq) * 256;
        gload8_sc1(ox0, (unsigned)(h * 512 + m * 8), oxb); gload8_sc1(ox1, (unsigned)(h * 512 + 256 + m * 8), oxb);
      } else {
        srdH[2] = 0u;
        nD = L_EX - L_G; nH = L_EX + 4096 - L_G - 8192;
      }
      const unsigned ldsd = ldsw + sl_dma;
      so = so0;
      unsigned cv;
      static_for<8>([&](auto ksc) {
        constexpr int ks = ksc;
        waitv<16>(); PIN(hf[ks]);
        if constexpr (ks == 5) arrive(aL, one);
        static_for<5>([&](auto tlc) {
          constexpr int tl = tlc, si = ks * 5 + tl;
          const bf16x8 wf = cat8(wlo[tl], whi[tl]);
          acc[tl] = MFMA(wf, BC(hf[ks]), acc[tl]);
          SBAR();
          if constexpr (si == 4) check(cv, aR, 4u * (j + 10));
          if constexpr (si == 35) { check(cv, aL, 4u * (j + 11)); r2d = r2d0 + nD; r2h = r2h0 + nH; TS(1, 14 + j); }
          constexpr int nk = (ks + 1) & 7, ko = (nk >> 1) * 9216;
          if constexpr (tl < 4) { wlo[tl] = trread(r2d + ko + (nk & 1) * 4096 + tl * 256); whi[tl] = trread(r2d + ko + (nk & 1) * 4096 + 2048 + tl * 256); }
          else { wlo[tl] = trread(r2h + ko + (nk & 1) * 512); whi[tl] = trread(r2h + ko + (nk & 1) * 512 + 256); }
          if constexpr (si == 0) cv = *(volatile LAS unsigned*)((lds_ptr)smem + L_SYNC + SY_R);
          if constexpr (si == 31) cv = *(volatile LAS unsigned*)((lds_ptr)smem + L_SYNC + SY_L);
          if constexpr (si == 34) arrive(aR, one);
          if constexpr (si >= 5 && si < 23 && (si & 1) == 1) { constexpr int p = (si - 5) >> 1; bdma16r<p * 1024>(p < 8 ? voffA : voffB, srdW, so, p == 4 ? sB4 : (p == 8 ? sB8 : sB), ldsd); }
          if constexpr (tl == 4) bload16_sc1<(ks & 1) * 1024>(hf[ks], voff, srdH, sh[ks >> 1]);
          SBAR();
        });
      });
      sl_dma = sl_cur; sl_cur = sl_nxt;
    }
    {
      PIN(ox0); PIN(ox1);
      const u32x4 ox = {ox0[0], ox0[1], ox1[0], ox1[1]};
#pragma unroll
      for (int tl = 0; tl < 5; ++tl) acc[tl] = MFMA(cat8(wlo[tl], whi[tl]), BC(ox), acc[tl]);
    }
    TS(1, 26);
    if (w != kq) {
      const __amdgpu_buffer_rsrc_t rsp = MKRS(WS_PART + (unsigned)((mt * 4 + nq) * 4 + kq) * 18 * 1024, 18 * 1024);
#pragma unroll
      for (int gi = 0; gi < 18; ++gi) {
        const f32x4 t = {acc[gi >> 2][4 * (gi & 3)], acc[gi >> 2][4 * (gi & 3) + 1], acc[gi >> 2][4 * (gi & 3) + 2], acc[gi >> 2][4 * (gi & 3) + 3]};
        __builtin_amdgcn_raw_buffer_store_b128(__builtin_bit_cast(u32x4, t), rsp, voff + gi * 1024, 0, 16);
      }
      WAITV(0);
      gsignal(flags + (FL_P + (mt * 4 + nq) * 128) / 4, lane);
    } else {
#pragma unroll
      for (int gi = 0; gi < 18; ++gi)
        *(LAS f32x4*)((lds_ptr)smem + L_KV + gi * 1024 + voff) = f32x4{acc[gi >> 2][4 * (gi & 3)], acc[gi >> 2][4 * (gi & 3) + 1], acc[gi >> 2][4 * (gi & 3) + 2], acc[gi >> 2][4 * (gi & 3) + 3]};
      lds_set(lds0 + L_SYNC + SY_F);
    }
    TS(1, 27);
  } else {
    TS(2, 0); HWID(2);
#define APRIO 1
    __builtin_amdgcn_s_setprio(APRIO);
    const char* kv = ws + WS_KV;
    const unsigned ldsk = lds0 + L_KV + 5 * w * 1024;
    const char* kvsrc = kv + REC + 5 * w * 1024;
    static_for<4>([&](auto p) { glds16<p * 1024, p * 1024>(voff, kvsrc, ldsk); });
    glds16<4096, 0>(voff, kvsrc + 4096, ldsk);
    const int t4 = tid & 255;
    const unsigned bv0 = b1[288 * cg + t4], bv1 = b1[288 * cg + 256 + (t4 & 31)];
    bf16x8 kA0 = *(const bf16x8*)(kv + lane * 16), kB0;
    u32x4 kA1 = {*(const unsigned*)(kv + 1024 + m * 4), 0x3f803f80u, 0x3f803f80u, 0u}, kB1 = {0u, 0x3f803f80u, 0x3f803f80u, 0u};
    {
      const int rho = 4 * w + ((lane >> 2) & 3);
      const int j = 4 * (rho >> 3) + (rho & 3), hh = (rho >> 2) & 1;
      const int grow = 18 * (8 * kq + 4 * hh + (j >> 1)) + 16 + (j & 1);
      glds16<L_EX, 0>((unsigned)((grow * D + 144 * nq + (lane >> 4) * 32 + (lane & 3) * 8) * 2), wo, lds0 + w * 1024);
      const int rho2 = (lane >> 1) & 15;
      const int j2 = 4 * (rho2 >> 3) + (rho2 & 3), hh2 = (rho2 >> 2) & 1;
      const int grow2 = 18 * (8 * kq + 4 * hh2 + (j2 >> 1)) + 16 + (j2 & 1);
      glds16<L_EX + 4096, 0>((unsigned)((grow2 * D + 144 * nq + 128 + (lane & 1) * 8) * 2), wo, lds0);
    }
    float* biasT = (float*)(smem + L_BIAS);
    biasT[t4] = __uint_as_float(bv0 << 16);
    biasT[256 + (t4 & 31)] = __uint_as_float(bv1 << 16);
    if (t4 < 16) *(unsigned*)(smem + L_SYNC + t4 * 4) = 0u;
    asm volatile("s_waitcnt lgkmcnt(0)\n\ts_barrier" ::: "memory");
    lds_wait(lds0 + L_SYNC + SY_Q + w * 4);
    TS(2, 11);
    const LAS unsigned* qrow = (const LAS unsigned*)((lds_ptr)smem + L_Q + (32 * w + m) * QROW + (36 * cg - n0q) * 2);
    u32x4 qa0 = {qrow[2 * h], qrow[2 * h + 1], qrow[4 + 2 * h], qrow[5 + 2 * h]};
    u32x4 qb0 = {qrow[9 + 2 * h], qrow[10 + 2 * h], qrow[13 + 2 * h], qrow[14 + 2 * h]};
    u32x4 qa1 = {qrow[8], 0u, 0u, 0u}, qb1 = {qrow[17], 0u, 0u, 0u};
    qa1[0] = h ? 0u : qa1[0]; qb1[0] = h ? 0u : qb1[0];
    bf16x8 vA0 = {}, vA1 = {}, vB0, vB1;
    f32x16 sa = MFMA(BC(kA1), BC(qa1), f32x16{}); sa = MFMA(kA0, BC(qa0), sa);
    f32x16 sb = MFMA(BC(kA1), BC(qb1), f32x16{}); sb = MFMA(kA0, BC(qb0), sb);
    float ma = __builtin_rintf(tile_max(sa)) + (float)M_OFF, mb = __builtin_rintf(tile_max(sb)) + (float)M_OFF;
    set_negm(qa1, ma, h); set_negm(qb1, mb, h);
#pragma unroll
    for (int r = 0; r < 16; ++r) sa[r] -= ma;
    f32x16 oa = {}, ob = {};
    u32x4 pa0, pa1, pb0 = {}, pb1 = {};
    bool bad = false;
    const int vr = m < DH ? m : DH;
    lds_ptr kp0 = (lds_ptr)smem + L_KV + lane * 16, kp1 = (lds_ptr)smem + L_KV + 1024 + m * 4, vp = (lds_ptr)smem + L_KV + 1152 + h * 304 + vr * 16;
    unsigned abar_addr = lds0 + L_SYNC + SY_A; PIN(abar_addr);
#define EX(v) __builtin_amdgcn_exp2f(v)
#define GAP(MF, SS, PW, i) do { MF; SBAR(); { const float e0 = EX(SS[4 * (i)]), e1 = EX(SS[4 * (i) + 1]), e2 = EX(SS[4 * (i) + 2]), e3 = EX(SS[4 * (i) + 3]); \
      PW[(2 * (i)) & 3] = cvtpk(e0, e1); PW[(2 * (i) + 1) & 3] = cvtpk(e2, e3); } PIN(PW); SBAR(); } while (0)
#define RESC(FL, O, SS, M, Q1) do { if (__builtin_expect(FL, 0)) { \
      bad |= !(O[10] < L_BAD); int e = __builtin_amdgcn_frexp_expf(O[10]) + M_OFF; e = e > 0 ? e : 0; const float ef = (float)e; \
      _Pragma("unroll") for (int r = 0; r < 16; ++r) { O[r] = __builtin_amdgcn_ldexpf(O[r], -e); SS[r] -= ef; } \
      M += ef; set_negm(Q1, M, h); } SBAR(); } while (0)
#define ITER(tt, KC0, KC1, VC0, VC1, KN0, KN1, VN0, VN1) do { \
    KN0 = *(const LAS bf16x8*)(kp0 + (tt) * REC); KN1[0] = *(const LAS unsigned*)(kp1 + (tt) * REC); \
    VN0 = *(const LAS bf16x8*)(vp + (tt) * REC); VN1 = *(const LAS bf16x8*)(vp + (tt) * REC + 608); SBAR(); \
    const bool flb = __any(!(ob[10] <= L_TRIG)); \
    GAP(sb = MFMA(BC(KC1), BC(qb1), f32x16{}), sa, pa0, 0); \
    GAP(sb = MFMA(KC0, BC(qb0), sb), sa, pa0, 1); \
    GAP(ob = MFMA(VC0, BC(pb0), ob), sa, pa1, 2); \
    GAP(ob = MFMA(VC1, BC(pb1), ob), sa, pa1, 3); \
    RESC(flb, ob, sb, mb, qb1); \
    const bool fla = __any(!(oa[10] <= L_TRIG)); \
    GAP(sa = MFMA(BC(KN1), BC(qa1), f32x16{}), sb, pb0, 0); \
    GAP(sa = MFMA(KN0, BC(qa0), sa), sb, pb0, 1); \
    GAP(oa = MFMA(VN0, BC(pa0), oa), sb, pb1, 2); \
    GAP(oa = MFMA(VN1, BC(pa1), oa), sb, pb1, 3); \
    RESC(fla, oa, sa, ma, qa1); \
  } while (0)
#pragma unroll 1
    for (int t2 = 0; t2 < 32; ++t2) {
      if ((t2 & 3) == 0) {
        const int c = t2 >> 2;
        swbar<0>(abar_addr, 4 * (c + 1));
        TS(2, 1 + c);
        if (c < 7) {
          const char* src = kvsrc + (size_t)(c + 1) * CHUNK; const unsigned dst = ldsk + ((c + 1) & 1) * CHUNK;
          static_for<4>([&](auto p) { glds16<p * 1024, p * 1024>(voff, src, dst); });
          glds16<4096, 0>(voff, src + 4096, dst);
        }
      }
      ITER(0, kA0, kA1, vA0, vA1, kB0, kB1, vB0, vB1);
      ITER(1, kB0, kB1, vB0, vB1, kA0, kA1, vA0, vA1);
      const int dlt = (t2 & 7) == 7 ? 2 * REC - 2 * CHUNK : 2 * REC;
      kp0 += dlt; kp1 += dlt; vp += dlt;
    }
    ob = MFMA(vA0, BC(pb0), ob); ob = MFMA(vA1, BC(pb1), ob);
#undef ITER
#undef RESC
#undef GAP
#undef EX
    {
      float ca = 0.f, cb = 0.f;
#pragma unroll
      for (int r = 0; r < 11; ++r) { ca = __builtin_fmaf(oa[r], 0.f, ca); cb = __builtin_fmaf(ob[r], 0.f, cb); }
      bad |= !(ca == 0.f) || !(cb == 0.f) || !(oa[10] < L_BAD) || !(ob[10] < L_BAD) || !(oa[10] > 1e-30f) || !(ob[10] > 1e-30f);
      if (__builtin_expect(__any(bad), 0)) { oa = attn_slow(kv, BC(qa0), qa1, lane); ob = attn_slow(kv, BC(qb0), qb1, lane); }
    }
    TS(2, 9);
    {
      const float ra = 1.0f / oa[10], rbb = 1.0f / ob[10];
      u32x4 fa, fb;
#pragma unroll
      for (int j = 0; j < 4; ++j) { fa[j] = cvtpk(oa[2 * j] * ra, oa[2 * j + 1] * ra); fb[j] = cvtpk(ob[2 * j] * rbb, ob[2 * j + 1] * rbb); }
      const __amdgpu_buffer_rsrc_t rso = MKRS(WS_OF + (unsigned)(mt * 32 + 2 * cg) * 1024, 2048), rsx = MKRS(WS_OX + (unsigned)(mt * 16 + cg) * 256, 256);
      __builtin_amdgcn_raw_buffer_store_b128(fa, rso, voff, 0, 16);
      __builtin_amdgcn_raw_buffer_store_b128(fb, rso, voff + 1024, 0, 16);
      const u32x2 ex = {cvtpk(oa[8] * ra, oa[9] * ra), cvtpk(ob[8] * rbb, ob[9] * rbb)};
      if (h == 0) __builtin_amdgcn_raw_buffer_store_b64(ex, rsx, m * 8, 0, 16);
      WAITV(0);
      gsignal(flags + (FL_O + (mt * 4 + kq) * 128) / 4, lane);
    }
    __builtin_amdgcn_s_setprio(0);
    TS(2, 10);
    {
      const int fm = rb * 4 + kq;
      const int p0 = w == 3 ? 6 : 2 * w, np = w == 3 ? 3 : 2;
      const size_t rowoff = (size_t)(32 * fm + m) * D + 144 * nq;
      u32x4 xv[3], bv[3];
#pragma unroll
      for (int i = 0; i < 3; ++i) { const int pi = i < np ? p0 + i : p0; xv[i] = *(const u32x4*)(x + rowoff + 16 * pi + 8 * h); bv[i] = *(const u32x4*)(b2 + 144 * nq + 16 * pi + 8 * h); }
      gpoll<8>(flags + (FL_P + (fm * 4 + nq) * 128) / 4, 3);
      TS(2, 12);
      const __amdgpu_buffer_rsrc_t rsp = MKRS(WS_PART + (unsigned)((fm * 4 + nq) * 4) * 18 * 1024, 4 * 18 * 1024);
      f32x4 s[6];
#pragma unroll
      for (int i = 0; i < 6; ++i) s[i] = f32x4{0.f, 0.f, 0.f, 0.f};
      u32x4 t[3][6];
#pragma unroll
      for (int q = 0; q < 3; ++q) {
        const int src = q < kq ? q : q + 1;
#pragma unroll
        for (int i = 0; i < 6; ++i) { const int gi = (i >> 1) < np ? 2 * p0 + i : 2 * p0; t[q][i] = __builtin_amdgcn_raw_buffer_load_b128(rsp, voff, (src * 18 + gi) * 1024, 16); }
      }
      lds_wait(lds0 + L_SYNC + SY_F);
#pragma unroll
      for (int i = 0; i < 6; ++i) {
        const int gi = (i >> 1) < np ? 2 * p0 + i : 2 * p0;
        const f32x4 own = *(const LAS f32x4*)((lds_ptr)smem + L_KV + gi * 1024 + voff);
        s[i] = ((__builtin_bit_cast(f32x4, t[0][i]) + __builtin_bit_cast(f32x4, t[1][i])) + __builtin_bit_cast(f32x4, t[2][i])) + own;
      }
#pragma unroll
      for (int i = 0; i < 3; ++i) {
        float a[4], b[4];
#pragma unroll
        for (int e = 0; e < 4; ++e) {
          auto r = __builtin_amdgcn_permlane32_swap(__float_as_uint(s[2 * i][e]), __float_as_uint(s[2 * i + 1][e]), false, false);
          a[e] = __uint_as_float(r[0]); b[e] = __uint_as_float(r[1]);
        }
        u32x4 o;
        o[0] = cvtpk(a[0] + bf_lo(xv[i][0]) + bf_lo(bv[i][0]), a[1] + bf_hi(xv[i][0]) + bf_hi(bv[i][0]));
        o[1] = cvtpk(a[2] + bf_lo(xv[i][1]) + bf_lo(bv[i][1]), a[3] + bf_hi(xv[i][1]) + bf_hi(bv[i][1]));
        o[2] = cvtpk(b[0] + bf_lo(xv[i][2]) + bf_lo(bv[i][2]), b[1] + bf_hi(xv[i][2]) + bf_hi(bv[i][2]));
        o[3] = cvtpk(b[2] + bf_lo(xv[i][3]) + bf_lo(bv[i][3]), b[3] + bf_hi(xv[i][3]) + bf_hi(bv[i][3]));
        if (i < np) *(u32x4*)(out + rowoff + 16 * (p0 + i) + 8 * h) = o;
      }
      TS(2, 13);
    }
  }
}

extern "C" void kernel_launch(void* const* d_in, const int* in_sizes, int n_in, void* d_out, int out_size, void* d_ws, size_t ws_size, hipStream_t stream) {
  const u16* x = (const u16*)d_in[0];
  const u16* wq = (const u16*)d_in[1];
  const u16* wk = (const u16*)d_in[2];
  const u16* wv = (const u16*)d_in[3];
  const u16* wo = (const u16*)d_in[4];
  const u16* w1 = (const u16*)d_in[6];
  const u16* b1 = (const u16*)d_in[7];
  const u16* w2 = (const u16*)d_in[8];
  const u16* b2 = (const u16*)d_in[9];
  char* ws = (char*)d_ws;
  (void)hipFuncSetAttribute((const void*)k_main, hipFuncAttributeMaxDynamicSharedMemorySize, L_TOTAL);
  k_ln_kv<<<64, 768, 0, stream>>>(x, wk, wv, ws);
  k_main<<<256, 512, L_TOTAL, stream>>>(x, wq, wo, w1, b1, w2, b2, ws, (u16*)d_out);
}
```

```cpp
#define G1PRIO 2
#define G2PRIO 0
#define K1AUX 16

#define G1SW 7
#include <hip/hip_runtime.h>
#include <hip/hip_bf16.h>
#include <stdint.h>
#include <type_traits>

typedef unsigned short u16;
using bf16x8 = __attribute__((ext_vector_type(8))) short;
using s16x4 = __attribute__((ext_vector_type(4))) short;
using f32x16 = __attribute__((ext_vector_type(16))) float;
using f32x4 = __attribute__((ext_vector_type(4))) float;
using u32x4 = __attribute__((ext_vector_type(4))) unsigned;
using u32x2 = __attribute__((ext_vector_type(2))) unsigned;
typedef float f32x2_t __attribute__((ext_vector_type(2)));
typedef __bf16 bf16x2_t __attribute__((ext_vector_type(2)));
#define LAS __attribute__((address_space(3)))
#define GAS __attribute__((address_space(1)))
typedef LAS char* lds_ptr;
typedef GAS unsigned gu32;
typedef short v4i16_t __attribute__((ext_vector_type(4)));

#define DI __device__ __forceinline__
#define SBAR() __builtin_amdgcn_sched_barrier(0)
#define PIN(x) asm volatile("" : "+v"(x))
#define PINS(x) asm volatile("" : "+s"(x))
#define MFMA(a, b, c) __builtin_amdgcn_mfma_f32_32x32x16_bf16((a), (b), (c), 0, 0, 0)
#define BC(x) __builtin_bit_cast(bf16x8, x)
#define RLX_AGENT __ATOMIC_RELAXED, __HIP_MEMORY_SCOPE_AGENT

constexpr int S = 2048, D = 576, H = 32, DH = 18, F = 4608;
constexpr float C2 = 0.23570226039551584f * 1.4426950408889634f;

constexpr unsigned WS_FLAGS = 0;
constexpr unsigned WS_XNF = 131072;
constexpr unsigned WS_KV = WS_XNF + 64 * 36 * 1024;
constexpr unsigned WS_OF = WS_KV + 196608;
constexpr unsigned WS_OX = WS_OF + 64 * 32 * 1024;
constexpr unsigned WS_HF = WS_OX + 64 * 16 * 256;
constexpr unsigned WS_PART = WS_HF + 64 * 288 * 1024;
constexpr unsigned WS_IMG = WS_PART + 64 * 16 * 18 * 1024;
constexpr unsigned IMG_CG = 117 * 1024;
constexpr unsigned WS_END = WS_IMG + 16 * IMG_CG;

constexpr int REC = 2560, CHUNK = 8 * REC;
constexpr int GSLOT = 36864;
constexpr int L_EX = 0, L_BIAS = 5120, L_SYNC = L_BIAS + 1152, L_G = 6400, L_KV = L_G + 3 * GSLOT, L_CNT = L_KV + 2 * CHUNK  , L_TOTAL = L_CNT + 768;
constexpr int L_Q = L_KV + CHUNK, QROW = 88;
constexpr int L_Q1 = L_Q + 128 * QROW;
constexpr int SY_R = 0, SY_A = 4, SY_Q = 8  , SY_F = 24, SY_L = 28, SY_L0 = 32;

DI unsigned cvtpk(float lo, float hi) { f32x2_t v = {lo, hi}; bf16x2_t b = __builtin_convertvector(v, bf16x2_t); return __builtin_bit_cast(unsigned, b); }
DI float bf_lo(unsigned u) { return __uint_as_float(u << 16); }
DI float bf_hi(unsigned u) { return __uint_as_float(u & 0xffff0000u); }
DI float bfr(float x) { return bf_lo(cvtpk(x, x) & 0xffffu); }
DI s16x4 trread(lds_ptr p) { return __builtin_bit_cast(s16x4, __builtin_amdgcn_ds_read_tr16_b64_v4i16((LAS v4i16_t*)p)); }
DI bf16x8 cat8(s16x4 a, s16x4 b) { return __builtin_shufflevector(a, b, 0, 1, 2, 3, 4, 5, 6, 7); }
template <int LI, int GI>
DI void glds16(unsigned voff, const void* sbase, unsigned ldsw) {
  asm volatile("s_add_u32 m0, %2, %3\n\ts_nop 0\n\tglobal_load_lds_dwordx4 %0, %1 offset:%4"
               :: "v"(voff), "s"(sbase), "s"(ldsw), "i"(LI - GI), "i"(GI) : "memory", "scc");
}
DI u32x4 mksrd(const void* p, unsigned bytes) { const unsigned long long a = (unsigned long long)p; return u32x4{(unsigned)a, (unsigned)(a >> 32) & 0xffffu, bytes, 0x00020000u}; }
template <int LI>
DI void bdma16(unsigned voff, u32x4 srd, unsigned soff, unsigned ldsw) {
  asm volatile("s_add_u32 m0, %3, %4\n\ts_nop 0\n\tbuffer_load_dwordx4 %0, %1, %2 offen lds"
               :: "v"(voff), "s"(srd), "s"(soff), "s"(ldsw), "i"(LI) : "memory", "scc");
}
template <int LI>
DI void bdma16r(unsigned voff, u32x4 srd, unsigned& soff, unsigned inc, unsigned ldsw) {
  asm volatile("s_add_u32 m0, %3, %4\n\ts_add_u32 %0, %0, %5\n\tbuffer_load_dwordx4 %1, %2, %0 offen lds"
               : "+s"(soff) : "v"(voff), "s"(srd), "s"(ldsw), "i"(LI), "s"(inc) : "memory", "scc");
}
template <int OFF>
DI void bload16_sc1(u32x4& dst, unsigned voff, u32x4 srd, unsigned soff) {
  asm volatile("buffer_load_dwordx4 %0, %1, %2, %3 offen offset:%4 sc1" : "=v"(dst) : "v"(voff), "s"(srd), "s"(soff), "i"(OFF) : "memory");
}
template <int OFF>
DI void gload16(bf16x8& dst, unsigned voff, const void* sbase) { asm volatile("global_load_dwordx4 %0, %1, %2 offset:%3" : "=v"(dst) : "v"(voff), "s"(sbase), "i"(OFF) : "memory"); }
template <int N> DI void waitv() { asm volatile("s_waitcnt vmcnt(%c0)" :: "i"(N) : "memory"); }
template <int OFF> DI void arrive(unsigned addr, unsigned one) { asm volatile("ds_add_u32 %0, %1 offset:%2" :: "v"(addr), "v"(one), "i"(OFF) : "memory"); }
template <int OFF> DI void check(unsigned v, unsigned addr, unsigned target) {
  asm volatile(
      "v_cmp_gt_u32 vcc, %2, %0\n\t"
      "s_cbranch_vccz 2f\n\t"
      "1:\n\t"
      "ds_read_b32 %0, %1 offset:%3\n\t"
      "s_waitcnt lgkmcnt(0)\n\t"
      "v_cmp_gt_u32 vcc, %2, %0\n\t"
      "s_cbranch_vccnz 1b\n\t"
      "2:"
      : "+v"(v) : "v"(addr), "s"(target), "i"(OFF) : "memory", "vcc");
}
constexpr int C_R = 0, C_L = 256, C_L0 = 512;
template <int LI> DI void set_m0(unsigned ldsw) { asm volatile("s_add_u32 m0, %0, %1" :: "s"(ldsw), "i"(LI) : "memory", "scc"); }
template <int OFF> DI void bdma_m0(unsigned voff, u32x4 srd, unsigned soff) {
  asm volatile("buffer_load_dwordx4 %0, %1, %2 offen offset:%3 lds" :: "v"(voff), "s"(srd), "s"(soff), "i"(OFF) : "memory");
}
DI void gload16_sc1(u32x4& dst, unsigned voff, const void* sbase) { asm volatile("global_load_dwordx4 %0, %1, %2 sc1" : "=v"(dst) : "v"(voff), "s"(sbase) : "memory"); }
DI void gload8_sc1(u32x2& dst, unsigned voff, const void* sbase) { asm volatile("global_load_dwordx2 %0, %1, %2 sc1" : "=v"(dst) : "v"(voff), "s"(sbase) : "memory"); }
#define WAITV(N) asm volatile("s_waitcnt vmcnt(" #N ")" ::: "memory")

#define TS(kid, i) do {} while (0)
#define HWID(kid) do {} while (0)

template <int VM>
DI void swbar(unsigned addr, unsigned target) {
  unsigned v, s;
  asm volatile(
      "s_waitcnt vmcnt(%c4) lgkmcnt(0)\n\t"
      "s_mov_b64 exec, 1\n\t"
      "v_mov_b32 %0, 1\n\t"
      "ds_add_rtn_u32 %0, %2, %0\n\t"
      "s_waitcnt lgkmcnt(0)\n\t"
      "v_add_u32 %0, 1, %0\n\t"
      "1:\n\t"
      "s_nop 0\n\t"
      "v_readfirstlane_b32 %1, %0\n\t"
      "s_nop 1\n\t"
      "s_cmp_ge_u32 %1, %3\n\t"
      "s_cbranch_scc1 2f\n\t"
      "ds_read_b32 %0, %2\n\t"
      "s_waitcnt lgkmcnt(0)\n\t"
      "s_branch 1b\n\t"
      "2:\n\t"
      "s_mov_b64 exec, -1"
      : "=&v"(v), "=&s"(s) : "v"(addr), "s"(target), "i"(VM) : "memory", "scc");
}
DI void lds_wait(unsigned addr) {
  unsigned v, s;
  asm volatile(
      "1:\n\t"
      "ds_read_b32 %0, %2\n\t"
      "s_waitcnt lgkmcnt(0)\n\t"
      "v_readfirstlane_b32 %1, %0\n\t"
      "s_nop 1\n\t"
      "s_cmp_lg_u32 %1, 0\n\t"
      "s_cbranch_scc1 2f\n\t"
      "s_sleep 2\n\t"
      "s_branch 1b\n\t"
      "2:"
      : "=&v"(v), "=&s"(s) : "v"(addr) : "memory", "scc");
}
DI void lds_set(unsigned addr) {
  unsigned v;
  asm volatile("s_waitcnt lgkmcnt(0)\n\tv_mov_b32 %0, 1\n\tds_write_b32 %1, %0" : "=&v"(v) : "v"(addr) : "memory");
}
DI void gpoll(gu32* p, unsigned need) {
#pragma unroll 1
  for (unsigned i = 0; i < (1u << 18); ++i) {
    if (((unsigned)__ballot(__hip_atomic_load(p, RLX_AGENT) != 0u) & need) == need) break;
    __builtin_amdgcn_s_sleep(1);
  }
  __builtin_amdgcn_fence(__ATOMIC_ACQUIRE, "wavefront");
  asm volatile("" ::: "memory");
}
DI void gsignal(gu32* p, bool on) { if (on) __hip_atomic_store(p, 1u, RLX_AGENT); }

#define CODELINES 248
#define PFN 2016
#define PFSLEEP 24
#define ST16(base, off, v) __builtin_amdgcn_raw_buffer_store_b128((v), __builtin_amdgcn_make_buffer_rsrc((void*)(base), 0, 1 << 20, 0x00020000), (off), 0, K1AUX)
#define M16(a, b, c) __builtin_amdgcn_mfma_f32_16x16x32_bf16((a), (b), (c), 0, 0, 0)
DI float xg_sum(float v) {
  auto r = __builtin_amdgcn_permlane16_swap(__float_as_uint(v), __float_as_uint(v), false, false);
  v = __uint_as_float(r[0]) + __uint_as_float(r[1]);
  auto q = __builtin_amdgcn_permlane32_swap(__float_as_uint(v), __float_as_uint(v), false, false);
  return __uint_as_float(q[0]) + __uint_as_float(q[1]);
}
extern "C" __global__ __launch_bounds__(384) void k_ln_kv(const u16* __restrict__ x, const u16* __restrict__ wk, const u16* __restrict__ wv, const u16* __restrict__ wq,
                                               const u16* __restrict__ w1, const u16* __restrict__ b1, const u16* __restrict__ w2, const u16* __restrict__ wo, char* __restrict__ ws) {
  __shared__ __attribute__((aligned(16))) char img[6 * 9216];
  __shared__ __attribute__((aligned(16))) float stats[16 * 12];
  const int tid = threadIdx.x, lane = tid & 63, m = lane & 15, g = lane >> 4;
  const int w = __builtin_amdgcn_readfirstlane(tid >> 6);
  unsigned cpf;
  {
    const unsigned long long pc = __builtin_amdgcn_s_getpc() & ~255ull;
    unsigned csz;
    asm volatile("s_mov_b32 %0, k_main-k_ln_kv+22528" : "=s"(csz));
    unsigned co = (unsigned)(blockIdx.x >= 128 ? tid : (tid & 63)) * 128u;
    co = co < csz ? co : 0u;
    asm volatile("global_load_dword %0, %1, %2" : "=v"(cpf) : "v"(co), "s"(pc) : "memory");
  }
  if (blockIdx.x >= 128) {
    const int pb = blockIdx.x - 128, kq = pb & 3, hf = (pb >> 2) & 1, idx = pb >> 3;
    const int cg = kq * 4 + (idx & 3), r0 = (hf * 4 + (idx >> 2)) * 72;
    if (tid < 256) TS(4, 0);
    u32x4 t[9];
    unsigned dst[3];
#pragma unroll
    for (int r = 0; r < 3; ++r) {
      const int c = tid + r * 384, cc = c < 936 ? c : 935, rr = cc / 13, k = cc - rr * 13, row = r0 + rr;
      const int sw = (row >> 2) & 2;
      const u16* src = k < 5 ? wq + row * D + ((36 * cg) & ~7) + k * 8 : w1 + (size_t)row * F + 288 * cg + (k - 5) * 8;
      t[r] = *(const u32x4*)src;
      dst[r] = k < 4 ? row * 64 + (k ^ sw) * 16 : (k == 4 ? 36864 + row * 16 : 46080 + ((k - 5) >> 2) * 36864 + row * 64 + (((k - 5) & 3) ^ sw) * 16);
    }
    __builtin_amdgcn_s_sleep(PFSLEEP);
#pragma unroll
    for (int r = 0; r < 6; ++r) { const int c = tid + r * 384, cc = c < PFN ? c : 0, rr = cc / 28; t[3 + r] = *(const u32x4*)(w1 + (size_t)(r0 + rr) * F + 288 * cg + 64 + (cc - rr * 28) * 8); }
    unsigned bpf = tid < 36 ? *(const unsigned*)(b1 + 288 * cg + tid * 8) : 0u;
#pragma unroll
    for (int r = 0; r < 3; ++r) ST16(ws + WS_IMG + (size_t)cg * IMG_CG, dst[r], t[r]);
#pragma unroll
    for (int r = 3; r < 9; ++r) asm volatile("" :: "v"(t[r]));
    asm volatile("" :: "v"(bpf));
    if (tid < 256) TS(4, 1);
    asm volatile("" :: "v"(cpf));
    return;
  }
  const int mt = blockIdx.x;
  if (tid < 256) { TS(0, 0); HWID(0); }
  const u16* xrow = x + (size_t)(16 * mt + m) * D;
  u32x4 xr[3];
#pragma unroll
  for (int i = 0; i < 3; ++i) xr[i] = *(const u32x4*)(xrow + 32 * (3 * w + i) + 8 * g);
  const float c0 = __uint_as_float((unsigned)xrow[0] << 16);
  u32x4 kc[4], vc[4];
#pragma unroll
  for (int i = 0; i < 4; ++i) { const int c = lane + 64 * i < 216 ? lane + 64 * i : 215; kc[i] = *(const u32x4*)((const char*)wk + w * 3456 + c * 16); vc[i] = *(const u32x4*)((const char*)wv + w * 3456 + c * 16); }
  if (tid < 64) *(unsigned*)(ws + WS_FLAGS + mt * 256 + tid * 4) = 0u;
  char* im = img + w * 9216;
#pragma unroll
  for (int i = 0; i < 4; ++i) {
    const int c = lane + 64 * i < 216 ? lane + 64 * i : 215;
#pragma unroll
    for (int j = 0; j < 4; ++j) {
      const int d = c * 4 + j, r = (d * 911) >> 13, o = d * 4 + r * 12;
      *(unsigned*)(im + o) = kc[i][j];
      *(unsigned*)(im + 4608 + o) = vc[i][j];
    }
  }
  float v[24], s = 0.f, q = 0.f;
#pragma unroll
  for (int i = 0; i < 3; ++i)
#pragma unroll
    for (int j = 0; j < 4; ++j) { v[8 * i + 2 * j] = bf_lo(xr[i][j]); v[8 * i + 2 * j + 1] = bf_hi(xr[i][j]); }
#pragma unroll
  for (int i = 0; i < 24; ++i) { const float d = v[i] - c0; s += d; q = __builtin_fmaf(d, d, q); }
  asm volatile("" : "+v"(s) : "v"(cpf));
  s = xg_sum(s); q = xg_sum(q);
  if (g == 0) *(f32x2_t*)&stats[m * 12 + 2 * w] = f32x2_t{s, q};
  const lds_ptr rd = (lds_ptr)im + (8 * g + (m >> 2)) * 48 + (m & 3) * 8, rd1 = (lds_ptr)im + (8 * g + (m >> 2)) * 48 + 32;
  bf16x8 kf[3][2], vf[3][2];
#pragma unroll
  for (int i = 0; i < 3; ++i) {
    kf[i][0] = cat8(trread(rd + i * 1536), trread(rd + i * 1536 + 192));
    kf[i][1] = cat8(trread(rd1 + i * 1536), trread(rd1 + i * 1536 + 192));
    vf[i][0] = cat8(trread(rd + 4608 + i * 1536), trread(rd + 4608 + i * 1536 + 192));
    vf[i][1] = cat8(trread(rd1 + 4608 + i * 1536), trread(rd1 + 4608 + i * 1536 + 192));
  }
  if (tid < 256) TS(0, 1);
  __syncthreads();
  if (tid < 256) TS(0, 2);
  s = 0.f; q = 0.f;
#pragma unroll
  for (int i = 0; i < 3; ++i) { const f32x4 t = *(const f32x4*)&stats[m * 12 + 4 * i]; s += t[0] + t[2]; q += t[1] + t[3]; }
  const float md = s * (1.f / 576.f);
  const float var = __builtin_fmaxf(__builtin_fmaf(-md, md, q * (1.f / 576.f)), 0.f);
  const float mb = bfr(c0 + md), sd = bfr(__builtin_amdgcn_sqrtf(bfr(bfr(var) + 1e-5f)));
  const float rinv = __builtin_amdgcn_rcpf(sd);
  f32x4 acc[4] = {};
#pragma unroll
  for (int i = 0; i < 3; ++i) {
    u32x4 o;
#pragma unroll
    for (int j = 0; j < 4; ++j) {
      const unsigned dp = cvtpk(v[8 * i + 2 * j] - mb, v[8 * i + 2 * j + 1] - mb);
      o[j] = cvtpk(bf_lo(dp) * rinv, bf_hi(dp) * rinv);
    }
    ST16(ws + WS_XNF + ((size_t)mt * 18 + 3 * w) * 1024, (unsigned)(i * 1024 + lane * 16), o);
    acc[0] = M16(kf[i][0], BC(o), acc[0]);
    acc[1] = M16(kf[i][1], BC(o), acc[1]);
    acc[2] = M16(BC(o), vf[i][0], acc[2]);
    acc[3] = M16(BC(o), vf[i][1], acc[3]);
  }
#pragma unroll
  for (int i = 0; i < 4; ++i) *(f32x4*)(im + i * 1024 + lane * 16) = acc[i];
  __syncthreads();
  if (tid < 256) TS(0, 3);
  if (w < 4) {
    f32x4 a = *(const f32x4*)(img + w * 1024 + lane * 16);
#pragma unroll
    for (int ww = 1; ww < 6; ++ww) a += *(const f32x4*)(img + ww * 9216 + w * 1024 + lane * 16);
    u32x2 p = {cvtpk(a[0], a[1]), cvtpk(a[2], a[3])};
    char* rec = ws + WS_KV + (size_t)(mt >> 1) * REC;
    const int key = 16 * (mt & 1) + m;
    if (w == 0) *(u32x2*)(rec + (key + 32 * (g & 1)) * 16 + (g >> 1) * 8) = p;
    else if (w == 1) { if (g == 0) *(unsigned*)(rec + 1024 + key * 4) = p[0]; }
    else {
      const int dc = (w - 2) * 16 + m;
      if (dc == DH) p = u32x2{0x3f803f80u, 0x3f803f80u};
      if (dc <= DH) *(u32x2*)(rec + REC + 1152 + ((mt & 1) * 2 + (g & 1)) * 304 + dc * 16 + (g >> 1) * 8) = p;
    }
  }
  if (tid < 256) TS(0, 4);
}

constexpr float L_TRIG = 9.5367431640625e-07f;
constexpr float L_BAD = 2.1267648e37f;
constexpr int M_OFF = 90;

DI void set_negm(u32x4& q1, float m, int h) {
  const float nm = -m;
  const unsigned hi = __float_as_uint(nm) & 0xffff0000u;
  const float r1 = nm - __uint_as_float(hi);
  const unsigned mid = __float_as_uint(r1) & 0xffff0000u;
  const float r2 = r1 - __uint_as_float(mid);
  q1[1] = h ? 0u : ((hi >> 16) | mid);
  q1[2] = h ? 0u : (__float_as_uint(r2) >> 16);
}
DI float half_max(float v) {
  auto rr = __builtin_amdgcn_permlane32_swap(__float_as_uint(v), __float_as_uint(v), false, false);
  return __builtin_fmaxf(__uint_as_float(rr[0]), __uint_as_float(rr[1]));
}
DI float tile_max(const f32x16& s) {
  float a = __builtin_fmaxf(s[0], s[1]);
#pragma unroll
  for (int r = 2; r < 16; ++r) a = __builtin_fmaxf(a, s[r]);
  return half_max(a);
}
DI f32x16 attn_slow(const char* kv, bf16x8 q0, u32x4 q1, int lane) {
  const int m = lane & 31, h = lane >> 5, vr = m < DH ? m : DH;
  q1[1] = 0u; q1[2] = 0u;
  float mx = -__builtin_inff();
  f32x16 o = f32x16{};
#pragma unroll 1
  for (int t = 0; t < 64; ++t) {
    const char* rk = kv + (size_t)t * REC; const char* rv = rk + REC + 1152 + h * 304 + vr * 16;
    const bf16x8 k0 = *(const bf16x8*)(rk + lane * 16);
    u32x4 k1 = {*(const unsigned*)(rk + 1024 + m * 4), 0u, 0u, 0u};
    const bf16x8 v0 = *(const bf16x8*)rv, v1 = *(const bf16x8*)(rv + 608);
    f32x16 s = MFMA(BC(k1), BC(q1), f32x16{});
    s = MFMA(k0, q0, s);
    const float mn = __builtin_fmaxf(mx, tile_max(s));
    const float al = __builtin_amdgcn_exp2f(mx - mn);
    mx = mn;
    u32x4 p0, p1;
#pragma unroll
    for (int j = 0; j < 4; ++j) {
      p0[j] = cvtpk(__builtin_amdgcn_exp2f(s[2 * j] - mn), __builtin_amdgcn_exp2f(s[2 * j + 1] - mn));
      p1[j] = cvtpk(__builtin_amdgcn_exp2f(s[8 + 2 * j] - mn), __builtin_amdgcn_exp2f(s[8 + 2 * j + 1] - mn));
    }
#pragma unroll
    for (int r = 0; r < 16; ++r) o[r] *= al;
    o = MFMA(v0, BC(p0), o);
    o = MFMA(v1, BC(p1), o);
  }
  return o;
}
DI unsigned relu2pk(float a, float b) {
  unsigned r; float t;
  asm volatile("v_mul_f32_e64 %0, %2, |%2|\n\tv_mul_f32_e64 %1, %3, |%3|\n\tv_cvt_pk_bf16_f32 %0, %0, %1\n\tv_pk_max_i16 %0, %0, 0" : "=&v"(r), "=&v"(t) : "v"(a), "v"(b));
  return r;
}
DI void mfma_dc(f32x4& d, bf16x8 a, bf16x8 b, const f32x4& c) { asm volatile("v_mfma_f32_16x16x32_bf16 %0, %1, %2, %3" : "=&v"(d) : "v"(a), "v"(b), "v"(c)); }
DI void mfma_acc(f32x4& c, bf16x8 a, bf16x8 b) { asm volatile("v_mfma_f32_16x16x32_bf16 %0, %1, %2, %0" : "+v"(c) : "v"(a), "v"(b)); }
DI void mfma_z(f32x4& d, bf16x8 a, bf16x8 b) { asm volatile("v_mfma_f32_16x16x32_bf16 %0, %1, %2, 0" : "=&v"(d) : "v"(a), "v"(b)); }
template <int OFF>
DI void bload8_sc1(u32x2& dst, unsigned voff, u32x4 srd) { asm volatile("buffer_load_dwordx2 %0, %1, %2, 0 offen offset:%3 sc1" : "=v"(dst) : "v"(voff), "s"(srd), "i"(OFF) : "memory"); }
DI float mul_abs(float a) { float r; asm("v_mul_f32_e64 %0, %1, |%1|" : "=v"(r) : "v"(a)); return r; }
DI unsigned pk_relu(unsigned p) { unsigned r; asm("v_pk_max_i16 %0, %1, 0" : "=v"(r) : "v"(p)); return r; }

#define ABL 0
#define ASLEEP 8
#define XPRE 24
template <int N, class Fn> DI void static_for(Fn&& f) {
  if constexpr (N > 0) { static_for<N - 1>(f); f(std::integral_constant<int, N - 1>{}); }
}

extern "C" __global__ __launch_bounds__(512, 2) void k_main(const u16* __restrict__ x, const u16* __restrict__ wq, const u16* __restrict__ wo, const u16* __restrict__ w1,
                                                 const u16* __restrict__ b1, const u16* __restrict__ w2, const u16* __restrict__ b2, char* __restrict__ ws, u16* __restrict__ out) {
  extern __shared__ __attribute__((aligned(16))) char smem[];
  const int xcd = blockIdx.x & 7, bidx = blockIdx.x >> 3;
  const int kq = xcd & 3, nq = bidx & 3, cg = kq * 4 + nq, rb = (xcd >> 2) * 8 + (bidx >> 2);
  const int tid = threadIdx.x, lane = tid & 63, m = lane & 31, h = lane >> 5;
  const int w8 = __builtin_amdgcn_readfirstlane(tid >> 6), w = w8 & 3;
  const int mt = rb * 4 + w;
  const unsigned lds0 = (unsigned)(uintptr_t)(lds_ptr)smem;
  const unsigned voff = lane * 16;
  const int n0q = (36 * cg) & ~7;
#define MKRS(off, bytes) __builtin_amdgcn_make_buffer_rsrc((void*)(ws + (off)), 0, (int)(bytes), 0x00020000)
  gu32* flags = (gu32*)(ws + WS_FLAGS);

  if (w8 < 4) {
    TS(1, 0); HWID(1);
    const int prow = lane >> 2, pch = (lane & 3) ^ ((lane >> 5) << 1);
    const int g4 = lane >> 4, i16 = lane & 15;
    const unsigned ldsw = lds0 + L_G + 9 * w * 1024;
    bf16x8 xf[36];
    const char* xb = ws + WS_XNF + (size_t)mt * 36 * 1024;
    auto xload = [&](auto uc) { constexpr int u = uc, o = ((u & 1) * 18 + (u >> 1)) * 1024; gload16<(o & 4095)>(xf[u], voff, xb + (o & ~4095)); };
    static_for<XPRE>([&](auto u) { xload(u); });
    u32x4 srdW = mksrd(w1, D * F * 2);
    unsigned so[9];
#pragma unroll
    for (int p = 0; p < 9; ++p) so[p] = (unsigned)((9 * w + p) * 16 * F * 2 - (p & 3) * 1024);
    unsigned voffA = (unsigned)((prow * F + pch * 8 + 288 * cg) * 2) + 64u, voffO, voffB;
    unsigned aC = lds0 + L_CNT + lane * 4, one = 1u, tgt = 4u;
    PIN(aC); PIN(one);
    lds_ptr pC = (lds_ptr)smem + L_CNT + lane * 4; PIN(pC);
    const int rdo = (8 * g4 + (i16 >> 2)) * 64 + (g4 & 1) * 32 + (i16 & 3) * 8;
    const lds_ptr rd0 = (lds_ptr)smem + L_G + rdo, rd1 = (lds_ptr)smem + L_G + (rdo ^ 32);
    lds_ptr biasp = (lds_ptr)smem + L_BIAS + g4 * 16;
    asm volatile("s_barrier" ::: "memory");
    TS(1, 1);
    __builtin_amdgcn_s_setprio(G1PRIO);
    s16x4 wr[18];
    f32x4 A[4], P[4], Bi[2];
    {
      const lds_ptr rq1 = (lds_ptr)smem + L_Q1 + (8 * g4 + (i16 >> 2)) * 16 + (i16 & 1) * 8;
      auto qread = [&](auto itc) {
        constexpr int it = itc, st = it / 6, tl = (it % 6) >> 1, hf = it & 1;
        if constexpr (st < 18) {
          if constexpr (tl == 0) wr[it % 12] = trread(rd0 + st * 2048 + hf * 256);
          else if constexpr (tl == 1) wr[it % 12] = trread(rd1 + st * 2048 + hf * 256);
          else wr[it % 12] = trread(rq1 + st * 512 + hf * 64);
        }
      };
      static_for<11>([&](auto it) { qread(it); });
      static_for<18>([&](auto ksc) {
        constexpr int ks = ksc;
        constexpr int issued = (XPRE + 6 * ks < 36 ? XPRE + 6 * ks : 36);
        waitv<issued - (2 * ks + 2)>();
        static_for<6>([&](auto sc) {
          constexpr int sI = sc, tl = sI >> 1, mi = sI & 1, gi = 6 * ks + sI;
          const bf16x8 wf = cat8(wr[(6 * ks + 2 * tl) % 12], wr[(6 * ks + 2 * tl + 1) % 12]);
          f32x4& Dd = tl < 2 ? A[sI & 3] : P[mi];
          if constexpr (ks == 0) mfma_z(Dd, wf, xf[2 * ks + mi]); else mfma_acc(Dd, wf, xf[2 * ks + mi]);
          SBAR();
          qread(std::integral_constant<int, gi + 11>{});
          if constexpr (gi + 11 == 107) arrive<C_R>(aC, one);
          if constexpr (XPRE + gi < 36) xload(std::integral_constant<int, (XPRE + gi < 36 ? XPRE + gi : 0)>{});
          SBAR();
        });
      });
      asm volatile("s_nop 7" : "+v"(A[0]), "+v"(A[1]), "+v"(A[2]), "+v"(A[3]), "+v"(P[0]), "+v"(P[1]));
      const lds_ptr qw = (lds_ptr)smem + L_Q + (32 * w + i16) * QROW + g4 * 8;
#pragma unroll
      for (int i = 0; i < 4; ++i) *(LAS u32x2*)(qw + (i & 1) * 16 * QROW + (i >> 1) * 32) = u32x2{cvtpk(A[i][0] * C2, A[i][1] * C2), cvtpk(A[i][2] * C2, A[i][3] * C2)};
      if (g4 < 2) {
#pragma unroll
        for (int i = 0; i < 2; ++i) *(LAS u32x2*)(qw + i * 16 * QROW + 64) = u32x2{cvtpk(P[i][0] * C2, P[i][1] * C2), cvtpk(P[i][2] * C2, P[i][3] * C2)};
      }
      lds_set(lds0 + L_SYNC + SY_Q + w * 4);
      P[2] = P[0]; P[3] = P[1];
      TS(1, 12);
      check<C_L0>(0u, aC, 4u);
    }
    TS(1, 2);
    const unsigned vA2 = (unsigned)((((lane >> 2) & 3) * D + (lane >> 4) * 32 + (lane & 3) * 8 + 144 * nq) * 2);
    const unsigned vO2 = (unsigned)((((lane >> 2) & 3) * D + (lane >> 4) * 32 + ((lane & 3) ^ 2) * 8 + 144 * nq) * 2);
    unsigned vB2 = (unsigned)(((lane >> 1) * D + 128 + (lane & 1) * 8 + 144 * nq) * 2);
    auto kpos = [&](int i) { int u = (i < 2 ? i + 7 : (i < 9 ? i - 2 : i)) + 9 * nq; return u >= 36 ? u - 36 : u; };
    const unsigned tabD = lane < 9 ? (unsigned)((1152 * kq + 32 * kpos(4 * lane + w)) * D * 2) : (unsigned)((144 * kq + 36 * w) * D * 2);
    const unsigned tabH = lane < 36 ? (unsigned)kpos(lane) * 2048u : (unsigned)(lane & 3) * 2048u;
    auto set_g2 = [&](int j) {
      const unsigned r = __builtin_amdgcn_readlane(tabD, j);
      voffA = vA2 + r; voffO = vO2 + r; voffB = vB2 + r;
    };
    auto bias_read = [&]() { Bi[0] = *(const LAS f32x4*)biasp; Bi[1] = *(const LAS f32x4*)(biasp + 64); biasp += 128; };
    const __amdgpu_buffer_rsrc_t rsh = MKRS(WS_HF + (unsigned)(mt * 288 + 18 * cg) * 1024, 18 * 1024);
    unsigned hst = voff + 16384u;
    unsigned ep[8] = {}, linc = 0u;
    int sl_cur = GSLOT, sl_dma = 0;
    lds_ptr ra = rd0 + GSLOT, rb1 = rd1 + GSLOT;
    auto wread1 = [&](auto itc) { constexpr int it = itc, st = (it >> 2) % 18; wr[it % 12] = trread(((it & 2) ? rb1 : ra) + st * 2048 + (it & 1) * 256); };
    auto dma_piece = [&](auto pc) { constexpr int p = pc; bdma_m0<(p & 3) * 1024>(p == 8 ? voffB : ((p & 1) ? voffO : voffA), srdW, so[p]); };
    auto pin_so = [&]() {
#pragma unroll
      for (int p = 0; p < 9; ++p) PINS(so[p]);
    };
    bias_read();
    static_for<11>([&](auto it) { wread1(it); });
#pragma unroll 1
    for (int t = 0; t < 9; ++t) {
      if (t == 7) {
        srdW = mksrd(w2, F * D * 2);
#pragma unroll
        for (int p = 0; p < 9; ++p) so[p] = p < 8 ? (unsigned)(p * 4 * D * 2 - (p & 3) * 1024) : 0u;
      }
      pin_so();
      if (t < 7) { voffA += 64; voffB = voffA; voffO = voffA; } else set_g2(t - 7);
      if (t == G1SW) __builtin_amdgcn_s_setprio(0);
      const unsigned ldsd = ldsw + sl_dma;
      const int sl_nxt = sl_cur == 2 * GSLOT ? 0 : sl_cur + GSLOT;
      unsigned cv;
      static_for<72>([&](auto gc) {
        constexpr int gi = gc, ks = gi >> 2, tl = (gi >> 1) & 1, mi = gi & 1;
        if constexpr ((gi & 3) == 0) asm volatile("" :: "v"(wr[gi % 12 + 3]));
        const bf16x8 wf = cat8(wr[(4 * ks + 2 * tl) % 12], wr[(4 * ks + 2 * tl + 1) % 12]);
        if constexpr (ks == 0) mfma_dc(A[gi & 3], wf, xf[2 * ks + mi], Bi[tl]);
        else if constexpr (ks == 17) mfma_dc(P[gi & 3], wf, xf[2 * ks + mi], A[gi & 3]);
        else mfma_acc(A[gi & 3], wf, xf[2 * ks + mi]);
        SBAR();
        if constexpr (gi == 11) { if constexpr (!(ABL & 8)) check<C_R>(cv, aC, tgt); tgt += 4u; }
        if constexpr (gi == 61) { if constexpr (!(ABL & 8)) check<C_L>(cv, aC, tgt); ra = rd0 + sl_nxt; rb1 = rd1 + sl_nxt; TS(1, 3 + t); }
        if constexpr (gi == 12 || gi == 40 || gi == 68) set_m0<(gi - 12) / 28 * 4096>(ldsd);
        if constexpr (!(ABL & 16)) wread1(std::integral_constant<int, gi + 11>{});
        if constexpr (gi == 0) cv = *(volatile LAS unsigned*)(pC + C_R);
        if constexpr (gi == 52) cv = *(volatile LAS unsigned*)(pC + C_L);
        if constexpr (gi == 60) arrive<C_R>(aC, one);
        if constexpr (!(ABL & 1) && gi >= 12 && (gi - 12) % 7 == 0) dma_piece(std::integral_constant<int, (gi - 12) / 7>{});
        if constexpr (!(ABL & 2) && gi >= 6 && gi <= 34 && gi % 4 == 2) {
          constexpr int j = (gi - 6) / 4, pi = ((j >> 1) & 1) * 2 + (j >> 2);
          ep[j] = relu2pk(P[pi][2 * (j & 1)], P[pi][2 * (j & 1) + 1]);
        }
        if constexpr (!(ABL & 4) && (gi == 23 || gi == 37)) {
          constexpr int hh = gi == 37;
          __builtin_amdgcn_raw_buffer_store_b128(u32x4{ep[4 * hh], ep[4 * hh + 1], ep[4 * hh + 2], ep[4 * hh + 3]}, rsh, hst + hh * 1024, 0, 16);
        }
        if constexpr (gi == 46) { if constexpr (!(ABL & 32)) waitv<7>(); arrive<C_L>(aC, linc); linc = 1u; PIN(linc); hst = voff + (unsigned)t * 2048u; PIN(hst); }
        if constexpr (gi == 44) bias_read();
        SBAR();
      });
      sl_dma = sl_cur; sl_cur = sl_nxt;
    }
    u32x4 hf[8];
    u32x4 srdH = mksrd(ws + WS_HF + ((size_t)mt * 288 + 72 * kq) * 1024, 72 * 1024);
    u32x4 srdX = mksrd(ws + WS_OX + (size_t)(mt * 16 + 4 * kq) * 256, 0);
    unsigned sh[4];
    auto set_h = [&](int j) {
#pragma unroll
      for (int i = 0; i < 4; ++i) sh[i] = __builtin_amdgcn_readlane(tabH, 4 * j + i);
    };
    set_h(0);
    asm volatile("s_nop 4" : "+s"(sh[2]), "+s"(sh[3]));
    static_for<4>([&](auto i) { bload16_sc1<(i & 1) * 1024>(hf[4 + i], voff, srdH, sh[2 + (i >> 1)]); });
    hf[0] = u32x4{ep[0], ep[1], ep[2], ep[3]}; hf[1] = u32x4{ep[4], ep[5], ep[6], ep[7]};
    const int r2o = g4 * 1024 + (i16 >> 2) * 64 + (g4 & 1) * 32 + (i16 & 3) * 8;
    const lds_ptr r2a0 = (lds_ptr)smem + L_G + r2o, r2b0 = (lds_ptr)smem + L_G + (r2o ^ 32);
    const lds_ptr r2h0 = (lds_ptr)smem + L_G + 8192 + (4 * g4 + (i16 >> 2)) * 32 + (i16 & 3) * 8;
    lds_ptr r2a = r2a0 + sl_cur, r2b = r2b0 + sl_cur, r2h = r2h0 + sl_cur;
    auto wread2 = [&](auto itc) {
      constexpr int it = itc, ks = (it / 18) & 3, nt = (it % 18) >> 1, hi = it & 1;
      if constexpr (nt < 8) wr[it % 18] = trread(((nt & 1) ? r2b : r2a) + ks * 9216 + hi * 4096 + (nt >> 1) * 256);
      else wr[it % 18] = trread(r2h + ks * 9216 + hi * 512);
    };
    static_for<17>([&](auto it) { wread2(it); });
    asm volatile("s_nop 7" : "+v"(P[0]), "+v"(P[1]), "+v"(P[2]), "+v"(P[3]));
#pragma unroll
    for (int j = 0; j < 8; ++j) { const int pi = ((j >> 1) & 1) * 2 + (j >> 2); ep[j] = relu2pk(P[pi][2 * (j & 1)], P[pi][2 * (j & 1) + 1]); }
    hf[2] = u32x4{ep[0], ep[1], ep[2], ep[3]}; hf[3] = u32x4{ep[4], ep[5], ep[6], ep[7]};
    __builtin_amdgcn_raw_buffer_store_b128(hf[2], rsh, hst, 0, 16);
    __builtin_amdgcn_raw_buffer_store_b128(hf[3], rsh, hst + 1024, 0, 16);
    f32x4 acc[18] = {};
    const unsigned voffX = (unsigned)(g4 * 256 + i16 * 8);
    gu32* fline = flags + (mt * 4 + kq) * 32;
    const unsigned foff = (unsigned)i16 * 4u;
    unsigned fl = 0u;
    TS(1, 13);
    __builtin_amdgcn_s_setprio(G2PRIO);
    u32x2 ox0, ox1;
#pragma unroll 1
    for (int j = 0; j < 10; ++j) {
      const int sl_nxt = sl_cur == 2 * GSLOT ? 0 : sl_cur + GSLOT;
      int nD = sl_nxt, nH = sl_nxt;
      unsigned need = 0u;
      if (j & 1) need = j < 6 ? 1u << ((nq + ((j + 1) >> 1)) & 3) : 0u;
      if (j >= 7) {
        if (j == 7) {
          srdW = mksrd(wo, D * D * 2);
#pragma unroll
          for (int p = 4; p < 8; ++p) so[p] += 2 * D * 2;
          vB2 += (unsigned)(h * 2 * D * 2);
        } else if (j == 8) {
          srdW[2] = 0u;
          need = 0xf0u | (0x100u << nq);
          srdH = mksrd(ws + WS_OF + ((size_t)mt * 32 + 8 * kq) * 1024, 8 * 1024);
        } else {
          srdH[2] = 0u; srdX[2] = 1024u;
          nD = L_EX - L_G; nH = L_EX + 4096 - L_G - 8192;
        }
      }
      pin_so();
      set_g2(j + 2); set_h(j + 1);
      const unsigned ldsd = ldsw + sl_dma;
      unsigned cv;
      static_for<72>([&](auto gc) {
        constexpr int gi = gc, ks = gi / 18, nt = (gi % 18) >> 1, mi = gi & 1;
        if constexpr (gi % 18 == 0) { waitv<(ks == 0 ? 14 : (ks == 1 ? 15 : (ks == 2 ? 13 : 16)))>(); if constexpr (ks == 2) arrive<C_L>(aC, one); }
        if constexpr (gi % 6 == 0) asm volatile("" :: "v"(wr[gi % 18 + 5]));
        const bf16x8 wf = cat8(wr[2 * nt], wr[2 * nt + 1]);
        mfma_acc(acc[gi % 18], wf, BC(hf[2 * ks + mi]));
        SBAR();
        if constexpr (gi == 5) { check<C_R>(cv, aC, tgt); tgt += 4u; }
        if constexpr (gi == 55) { check<C_L>(cv, aC, tgt); r2a = r2a0 + nD; r2b = r2b0 + nD; r2h = r2h0 + nH; TS(1, 14 + j); }
        constexpr int dp = gi == 6 ? 0 : gi == 11 ? 1 : gi == 21 ? 2 : gi == 26 ? 3 : gi == 31 ? 4 : gi == 40 ? 5 : gi == 46 ? 6 : gi == 58 ? 7 : gi == 64 ? 8 : -1;
        if constexpr (dp >= 0 && (dp & 3) == 0) set_m0<dp * 1024>(ldsd);
        wread2(std::integral_constant<int, gi + 17>{});
        if constexpr (gi == 0) cv = *(volatile LAS unsigned*)(pC + C_R);
        if constexpr (gi == 44) cv = *(volatile LAS unsigned*)(pC + C_L);
        if constexpr (gi == 54) arrive<C_R>(aC, one);
        if constexpr (gi == 2) bload8_sc1<0>(ox0, voffX, srdX);
        if constexpr (gi == 3) bload8_sc1<128>(ox1, voffX, srdX);
        if constexpr (dp >= 0) dma_piece(std::integral_constant<int, (dp >= 0 ? dp : 0)>{});
        if constexpr (gi == 15) {
          if (need) {
            waitv<10>(); PIN(fl);
            if (((unsigned)__ballot(fl != 0u) & need) != need) { gpoll(fline + i16, need); TS(1, 24); }
          }
        }
        if constexpr (gi == 49) asm volatile("global_load_dword %0, %1, %2 sc1" : "+v"(fl) : "v"(foff), "s"(fline) : "memory");
        if constexpr (gi % 18 >= 16) bload16_sc1<mi * 1024>(hf[2 * ks + mi], voff, srdH, sh[ks]);
        if constexpr (gi == 24) { if (j == 0) { waitv<7>(); gsignal(fline + nq, lane == 0); } }
        SBAR();
      });
      sl_dma = sl_cur; sl_cur = sl_nxt;
    }
    waitv<0>();
    asm volatile("s_nop 7" : "+v"(ox0), "+v"(ox1) : "v"(fl), "v"(hf[0]), "v"(hf[1]), "v"(hf[2]), "v"(hf[3]), "v"(hf[4]), "v"(hf[5]), "v"(hf[6]), "v"(hf[7]));
#pragma unroll
    for (int i = 0; i < 18; ++i) acc[i] = __builtin_amdgcn_mfma_f32_16x16x16bf16_1k(wr[i & ~1], __builtin_bit_cast(s16x4, (i & 1) ? ox1 : ox0), acc[i], 0, 0, 0);
    TS(1, 26);
    if (w != kq) {
      const __amdgpu_buffer_rsrc_t rsp = MKRS(WS_PART + (unsigned)((mt * 4 + nq) * 4 + kq) * 9 * 1024, 9 * 1024);
#pragma unroll
      for (int nt = 0; nt < 9; ++nt) {
        auto r0 = __builtin_amdgcn_permlane16_swap(cvtpk(acc[2 * nt][0], acc[2 * nt][1]), cvtpk(acc[2 * nt + 1][0], acc[2 * nt + 1][1]), false, false);
        auto r1 = __builtin_amdgcn_permlane16_swap(cvtpk(acc[2 * nt][2], acc[2 * nt][3]), cvtpk(acc[2 * nt + 1][2], acc[2 * nt + 1][3]), false, false);
        __builtin_amdgcn_raw_buffer_store_b128(u32x4{r0[0], r1[0], r0[1], r1[1]}, rsp, voff + nt * 1024, 0, 16);
      }
    } else {
#pragma unroll
      for (int gi = 0; gi < 18; ++gi) *(LAS f32x4*)((lds_ptr)smem + L_KV + gi * 1024 + voff) = acc[gi];
      lds_set(lds0 + L_SYNC + SY_F);
    }
    TS(1, 27);
  } else {
    TS(2, 0); HWID(2);
#define APRIO 1
    __builtin_amdgcn_s_setprio(APRIO);
    const char* kv = ws + WS_KV;
    const unsigned ldsk = lds0 + L_KV + 5 * w * 1024;
    const char* kvsrc = kv + REC + 5 * w * 1024;
    const int t4 = tid & 255;
    const unsigned bv0 = b1[288 * cg + t4], bv1 = b1[288 * cg + 256 + (t4 & 31)];
    {
      const u32x4 srdI = mksrd(ws + WS_IMG + (size_t)cg * IMG_CG, IMG_CG);
      const unsigned ldsw = lds0 + L_G + 9 * w * 1024, si = (unsigned)(9 * w * 1024);
      static_for<9>([&](auto p) { bdma16<p * 1024>(voff, srdI, si + p * 1024, ldsw); });
      bdma16<L_Q1>(voff, srdI, 36864u + w * 1024, lds0 + w * 1024);
      bdma16<L_Q1 + 4096>(voff, srdI, 36864u + 4096 + w * 1024, lds0 + w * 1024);
      bdma16<L_Q1 + 8192>(voff, srdI, 36864u + 8192, lds0);
      if (t4 < 16) *(unsigned*)(smem + L_SYNC + t4 * 4) = 0u;
      if (t4 < 192) *(unsigned*)(smem + L_CNT + t4 * 4) = 0u;
      asm volatile("s_waitcnt vmcnt(0) lgkmcnt(0)\n\ts_barrier" ::: "memory");
      float* biasT = (float*)(smem + L_BIAS);
      biasT[t4] = __uint_as_float(bv0 << 16);
      biasT[256 + (t4 & 31)] = __uint_as_float(bv1 << 16);
      __builtin_amdgcn_s_sleep(ASLEEP);
      static_for<9>([&](auto p) { bdma16<GSLOT + p * 1024>(voff, srdI, 46080u + si + p * 1024, ldsw); });
      static_for<4>([&](auto p) { glds16<p * 1024, p * 1024>(voff, kvsrc, ldsk); });
      glds16<4096, 0>(voff, kvsrc + 4096, ldsk);
      static_for<9>([&](auto p) { bdma16<2 * GSLOT + p * 1024>(voff, srdI, 46080u + 36864u + si + p * 1024, ldsw); });
    }
    bf16x8 kA0 = *(const bf16x8*)(kv + lane * 16), kB0;
    u32x4 kA1 = {*(const unsigned*)(kv + 1024 + m * 4), 0x3f803f80u, 0x3f803f80u, 0u}, kB1 = {0u, 0x3f803f80u, 0x3f803f80u, 0u};
    waitv<16>();
    const unsigned aC = lds0 + L_CNT + lane * 4;
    arrive<C_L0>(aC, 1u);
    lds_wait(lds0 + L_SYNC + SY_Q + w * 4);
    TS(2, 11);
    const LAS unsigned* qrow = (const LAS unsigned*)((lds_ptr)smem + L_Q + (32 * w + m) * QROW + (36 * cg - n0q) * 2);
    u32x4 qa0 = {qrow[2 * h], qrow[2 * h + 1], qrow[4 + 2 * h], qrow[5 + 2 * h]};
    u32x4 qb0 = {qrow[9 + 2 * h], qrow[10 + 2 * h], qrow[13 + 2 * h], qrow[14 + 2 * h]};
    u32x4 qa1 = {qrow[8], 0u, 0u, 0u}, qb1 = {qrow[17], 0u, 0u, 0u};
    qa1[0] = h ? 0u : qa1[0]; qb1[0] = h ? 0u : qb1[0];
    bf16x8 vA0 = {}, vA1 = {}, vB0, vB1;
    f32x16 sa = MFMA(BC(kA1), BC(qa1), f32x16{}); sa = MFMA(kA0, BC(qa0), sa);
    f32x16 sb = MFMA(BC(kA1), BC(qb1), f32x16{}); sb = MFMA(kA0, BC(qb0), sb);
    float ma = __builtin_rintf(tile_max(sa)) + (float)M_OFF, mb = __builtin_rintf(tile_max(sb)) + (float)M_OFF;
    set_negm(qa1, ma, h); set_negm(qb1, mb, h);
#pragma unroll
    for (int r = 0; r < 16; ++r) sa[r] -= ma;
    f32x16 oa = {}, ob = {};
    u32x4 pa0, pa1, pb0 = {}, pb1 = {};
    PIN(kA1); PIN(kB1);
    const int vr = m < DH ? m : DH;
    lds_ptr kp0 = (lds_ptr)smem + L_KV + lane * 16, kp1 = (lds_ptr)smem + L_KV + 1024 + m * 4, vp = (lds_ptr)smem + L_KV + 1152 + h * 304 + vr * 16;
    unsigned abar_addr = lds0 + L_SYNC + SY_A; PIN(abar_addr);
    const __amdgpu_buffer_rsrc_t rsp = MKRS(WS_PART + (unsigned)(((rb * 4 + kq) * 4 + nq) * 4) * 9 * 1024, 4 * 9 * 1024);
#define EX(v) __builtin_amdgcn_exp2f(v)
#define GAP(MF, SS, PW, i) do { MF; SBAR(); { const float e0 = EX(SS[4 * (i)]), e1 = EX(SS[4 * (i) + 1]), e2 = EX(SS[4 * (i) + 2]), e3 = EX(SS[4 * (i) + 3]); \
      PW[(2 * (i)) & 3] = cvtpk(e0, e1); PW[(2 * (i) + 1) & 3] = cvtpk(e2, e3); } PIN(PW); SBAR(); } while (0)
#define RESC(FL, O, SS, M, Q1) do { if (__builtin_expect(FL, 0)) { \
      int e = __builtin_amdgcn_frexp_expf(O[10]) + M_OFF; e = e > 0 ? e : 0; const float ef = (float)e; \
      _Pragma("unroll") for (int r = 0; r < 16; ++r) { O[r] = __builtin_amdgcn_ldexpf(O[r], -e); SS[r] -= ef; } \
      M += ef; set_negm(Q1, M, h); } SBAR(); } while (0)
#define ITER(tt, CK, KC0, KC1, VC0, VC1, KN0, KN1, VN0, VN1) do { \
    bool flb = false, fla = false; \
    GAP(sb = MFMA(BC(KC1), BC(qb1), f32x16{}); SBAR(); KN1[0] = *(const LAS unsigned*)(kp1 + (tt) * REC); KN0 = *(const LAS bf16x8*)(kp0 + (tt) * REC); if (CK) flb = __any(!(ob[10] <= L_TRIG)), sa, pa0, 0); \
    GAP(sb = MFMA(KC0, BC(qb0), sb); SBAR(); VN0 = *(const LAS bf16x8*)(vp + (tt) * REC); VN1 = *(const LAS bf16x8*)(vp + (tt) * REC + 608), sa, pa0, 1); \
    GAP(ob = MFMA(VC0, BC(pb0), ob), sa, pa1, 2); \
    GAP(ob = MFMA(VC1, BC(pb1), ob), sa, pa1, 3); \
    if (CK) RESC(flb, ob, sb, mb, qb1); \
    asm volatile("" :: "v"(VN1)); \
    GAP(sa = MFMA(BC(KN1), BC(qa1), f32x16{}); SBAR(); if (CK) fla = __any(!(oa[10] <= L_TRIG)), sb, pb0, 0); \
    GAP(sa = MFMA(KN0, BC(qa0), sa), sb, pb0, 1); \
    GAP(oa = MFMA(VN0, BC(pa0), oa), sb, pb1, 2); \
    GAP(oa = MFMA(VN1, BC(pa1), oa), sb, pb1, 3); \
    if (CK) RESC(fla, oa, sa, ma, qa1); \
  } while (0)
#pragma unroll 1
    for (int c = 0; c < 8; ++c) {
      {
        if (c >= 2 && c <= 4 && w != kq) swbar<3>(abar_addr, 4 * (c + 1)); else swbar<0>(abar_addr, 4 * (c + 1));
        TS(2, 1 + c);
        if (c < 7) {
          const char* src = kvsrc + (size_t)(c + 1) * CHUNK; const unsigned dst = ldsk + ((c + 1) & 1) * CHUNK;
          static_for<4>([&](auto p) { glds16<p * 1024, p * 1024>(voff, src, dst); });
          glds16<4096, 0>(voff, src + 4096, dst);
        }
        if (c == 0) arrive<C_L>(aC, 2u);
        else if (c <= 3) {
          if (w != kq) {
#pragma unroll
            for (int i = 0; i < 3; ++i) __builtin_amdgcn_raw_buffer_store_b128(u32x4{~0u, ~0u, ~0u, ~0u}, rsp, voff + (unsigned)(w * 9216 + (c - 1) * 3072) + i * 1024, 0, 16);
          }
        } else if (c == 4) {
          const int r = (lane >> 2) & 3;
          const int grow = 36 * (4 * kq + w) + 16 + (r >> 1) * 18 + (r & 1);
          glds16<L_EX, 0>((unsigned)((grow * D + 144 * nq + (lane >> 4) * 32 + ((lane & 3) ^ ((w & 1) << 1)) * 8) * 2), wo, lds0 + w * 1024);
          const int rho = (lane >> 1) & 15;
          const int grow2 = 36 * (4 * kq + (rho >> 2)) + 16 + ((rho >> 1) & 1) * 18 + (rho & 1);
          glds16<L_EX + 4096, 0>((unsigned)((grow2 * D + 144 * nq + 128 + (lane & 1) * 8) * 2), wo, lds0);
        } else if (c == 5) gsignal(flags + ((rb * 4 + kq) * 4 + (lane & 3)) * 32 + 8 + nq, w == 0 && lane < 4);
      }
#pragma unroll
      for (int t2 = 0; t2 < 4; ++t2) {
        ITER(2 * t2, false, kA0, kA1, vA0, vA1, kB0, kB1, vB0, vB1);
        ITER(2 * t2 + 1, true, kB0, kB1, vB0, vB1, kA0, kA1, vA0, vA1);
      }
      const int dlt = (c & 1) ? -CHUNK : CHUNK;
      kp0 += dlt; kp1 += dlt; vp += dlt;
    }
    ob = MFMA(vA0, BC(pb0), ob); ob = MFMA(vA1, BC(pb1), ob);
#undef ITER
#undef RESC
#undef GAP
#undef EX
    {
      float ca = 0.f, cb = 0.f;
#pragma unroll
      for (int r = 0; r < 11; ++r) { ca = __builtin_fmaf(oa[r], 0.f, ca); cb = __builtin_fmaf(ob[r], 0.f, cb); }
      const bool bad = !(ca == 0.f) || !(cb == 0.f) || !(oa[10] > 1e-29f) || !(ob[10] > 1e-29f);
      if (__builtin_expect(__any(bad), 0)) { oa = attn_slow(kv, BC(qa0), qa1, lane); ob = attn_slow(kv, BC(qb0), qb1, lane); }
    }
    TS(2, 9);
    {
      const float ra = 1.0f / oa[10], rbb = 1.0f / ob[10];
      const u32x4 f0 = {cvtpk(oa[0] * ra, oa[1] * ra), cvtpk(oa[2] * ra, oa[3] * ra), cvtpk(ob[0] * rbb, ob[1] * rbb), cvtpk(ob[2] * rbb, ob[3] * rbb)};
      const u32x4 f1 = {cvtpk(oa[4] * ra, oa[5] * ra), cvtpk(oa[6] * ra, oa[7] * ra), cvtpk(ob[4] * rbb, ob[5] * rbb), cvtpk(ob[6] * rbb, ob[7] * rbb)};
      const __amdgpu_buffer_rsrc_t rso = MKRS(WS_OF + (unsigned)(mt * 32 + 2 * cg) * 1024, 2048), rsx = MKRS(WS_OX + (unsigned)(mt * 16 + cg) * 256, 256);
      const unsigned oo = (unsigned)((m >> 4) * 1024 + ((m & 15) + 16 * h) * 16);
      __builtin_amdgcn_raw_buffer_store_b128(f0, rso, oo, 0, 16);
      __builtin_amdgcn_raw_buffer_store_b128(f1, rso, oo + 512, 0, 16);
      const u32x2 ex = {cvtpk(oa[8] * ra, oa[9] * ra), cvtpk(ob[8] * rbb, ob[9] * rbb)};
      if (h == 0) __builtin_amdgcn_raw_buffer_store_b64(ex, rsx, m * 8, 0, 16);
      WAITV(0);
      gsignal(flags + (mt * 4 + kq) * 32 + 4 + nq, lane == 0);
    }
    __builtin_amdgcn_s_setprio(0);
    TS(2, 10);
    {
      const int fm = rb * 4 + kq;
      const int p0 = w == 3 ? 6 : 2 * w, np = w == 3 ? 3 : 2;
      const int g4 = lane >> 4;
      const size_t rowoff = (size_t)(32 * fm + 16 * (g4 & 1) + (lane & 15)) * D + 144 * nq;
      u32x4 xv[3], bv[3];
#pragma unroll
      for (int i = 0; i < 3; ++i) { const int pi = i < np ? p0 + i : p0; xv[i] = *(const u32x4*)(x + rowoff + 16 * pi + 8 * h); bv[i] = *(const u32x4*)(b2 + 144 * nq + 16 * pi + 8 * h); }
      lds_wait(lds0 + L_SYNC + SY_F);
      TS(2, 12);
      u32x4 t[3][3];
      auto pload = [&]() {
#pragma unroll
        for (int q = 0; q < 3; ++q) {
          const int src = q < kq ? q : q + 1;
#pragma unroll
          for (int i = 0; i < 3; ++i) { const int pi = i < np ? p0 + i : p0; t[q][i] = __builtin_amdgcn_raw_buffer_load_b128(rsp, voff, (src * 9 + pi) * 1024, 16); }
        }
      };
      pload();
      SBAR();
      float base[3][8];
#pragma unroll
      for (int i = 0; i < 3; ++i) {
        const int pi = i < np ? p0 + i : p0;
        const f32x4 o0 = *(const LAS f32x4*)((lds_ptr)smem + L_KV + pi * 2048 + voff), o1 = *(const LAS f32x4*)((lds_ptr)smem + L_KV + pi * 2048 + 1024 + voff);
#pragma unroll
        for (int e = 0; e < 4; ++e) {
          auto r = __builtin_amdgcn_permlane16_swap(__float_as_uint(o0[e]), __float_as_uint(o1[e]), false, false);
          const unsigned xa = xv[i][e >> 1], ba = bv[i][e >> 1], xb = xv[i][2 + (e >> 1)], bb = bv[i][2 + (e >> 1)];
          base[i][e] = __uint_as_float(r[0]) + ((e & 1) ? bf_hi(xa) + bf_hi(ba) : bf_lo(xa) + bf_lo(ba));
          base[i][4 + e] = __uint_as_float(r[1]) + ((e & 1) ? bf_hi(xb) + bf_hi(bb) : bf_lo(xb) + bf_lo(bb));
        }
      }
      SBAR();
#pragma unroll 1
      for (int it = 0; it < (1 << 14); ++it) {
        unsigned mx = 0u;
#pragma unroll
        for (int q = 0; q < 3; ++q)
#pragma unroll
          for (int i = 0; i < 3; ++i)
#pragma unroll
            for (int e = 0; e < 4; ++e) mx = mx > t[q][i][e] ? mx : t[q][i][e];
        if (!__any(mx == ~0u)) break;
        pload();
      }
#pragma unroll
      for (int i = 0; i < 3; ++i) {
        u32x4 o;
#pragma unroll
        for (int e = 0; e < 4; ++e)
          o[e] = cvtpk(((base[i][2 * e] + bf_lo(t[0][i][e])) + bf_lo(t[1][i][e])) + bf_lo(t[2][i][e]), ((base[i][2 * e + 1] + bf_hi(t[0][i][e])) + bf_hi(t[1][i][e])) + bf_hi(t[2][i][e]));
        if (i < np) *(u32x4*)(out + rowoff + 16 * (p0 + i) + 8 * h) = o;
      }
      TS(2, 13);
    }
  }
}

extern "C" void kernel_launch(void* const* d_in, const int* in_sizes, int n_in, void* d_out, int out_size, void* d_ws, size_t ws_size, hipStream_t stream) {
  const u16* x = (const u16*)d_in[0];
  const u16* wq = (const u16*)d_in[1];
  const u16* wk = (const u16*)d_in[2];
  const u16* wv = (const u16*)d_in[3];
  const u16* wo = (const u16*)d_in[4];
  const u16* w1 = (const u16*)d_in[6];
  const u16* b1 = (const u16*)d_in[7];
  const u16* w2 = (const u16*)d_in[8];
  const u16* b2 = (const u16*)d_in[9];
  char* ws = (char*)d_ws;
  (void)hipFuncSetAttribute((const void*)k_main, hipFuncAttributeMaxDynamicSharedMemorySize, L_TOTAL);
  k_ln_kv<<<256, 384, 0, stream>>>(x, wk, wv, wq, w1, b1, w2, wo, ws);
  k_main<<<256, 512, L_TOTAL, stream>>>(x, wq, wo, w1, b1, w2, b2, ws, (u16*)d_out);
}
```

```cpp
#include <hip/hip_runtime.h>
#include <hip/hip_bf16.h>
#include <stdint.h>
#include <type_traits>

typedef unsigned short u16;
using bf16x8 = __attribute__((ext_vector_type(8))) short;
using s16x4 = __attribute__((ext_vector_type(4))) short;
using f32x16 = __attribute__((ext_vector_type(16))) float;
using f32x4 = __attribute__((ext_vector_type(4))) float;
using u32x4 = __attribute__((ext_vector_type(4))) unsigned;
using u32x2 = __attribute__((ext_vector_type(2))) unsigned;
typedef float f32x2_t __attribute__((ext_vector_type(2)));
typedef __bf16 bf16x2_t __attribute__((ext_vector_type(2)));
#define LAS __attribute__((address_space(3)))
#define GAS __attribute__((address_space(1)))
typedef LAS char* lds_ptr;
typedef GAS unsigned gu32;
typedef short v4i16_t __attribute__((ext_vector_type(4)));

#define DI __device__ __forceinline__
#define SBAR() __builtin_amdgcn_sched_barrier(0)
#define PIN(x) asm volatile("" : "+v"(x))
#define PINS(x) asm volatile("" : "+s"(x))
#define MFMA(a, b, c) __builtin_amdgcn_mfma_f32_32x32x16_bf16((a), (b), (c), 0, 0, 0)
#define BC(x) __builtin_bit_cast(bf16x8, x)
#define RLX_AGENT __ATOMIC_RELAXED, __HIP_MEMORY_SCOPE_AGENT

constexpr int D = 576, DH = 18, F = 4608;
constexpr float C2 = 0.23570226039551584f * 1.4426950408889634f;

constexpr unsigned WS_FLAGS = 0;
constexpr unsigned WS_XNF = 131072;
constexpr unsigned WS_KV = WS_XNF + 64 * 36 * 1024;
constexpr unsigned WS_OF = WS_KV + 196608;
constexpr unsigned WS_OX = WS_OF + 64 * 32 * 1024;
constexpr unsigned WS_HF = WS_OX + 64 * 16 * 256;
constexpr unsigned WS_PART = WS_HF + 64 * 288 * 1024;
constexpr unsigned WS_IMG = WS_PART + 64 * 16 * 18 * 1024;
constexpr unsigned IMG_CG = 117 * 1024;

constexpr int REC = 2560, CHUNK = 8 * REC;
constexpr int GSLOT = 36864;
constexpr int L_EX = 0, L_BIAS = 5120, L_SYNC = L_BIAS + 1152, L_G = 6400, L_KV = L_G + 3 * GSLOT, L_CNT = L_KV + 2 * CHUNK  , L_TOTAL = L_CNT + 768;
constexpr int L_Q = L_KV + CHUNK, QROW = 88;
constexpr int L_Q1 = L_Q + 128 * QROW;
constexpr int SY_A = 4, SY_Q = 8  , SY_F = 24;

DI unsigned cvtpk(float lo, float hi) { f32x2_t v = {lo, hi}; bf16x2_t b = __builtin_convertvector(v, bf16x2_t); return __builtin_bit_cast(unsigned, b); }
DI float bf_lo(unsigned u) { return __uint_as_float(u << 16); }
DI float bf_hi(unsigned u) { return __uint_as_float(u & 0xffff0000u); }
DI float bfr(float x) { return bf_lo(cvtpk(x, x) & 0xffffu); }
DI s16x4 trread(lds_ptr p) { return __builtin_bit_cast(s16x4, __builtin_amdgcn_ds_read_tr16_b64_v4i16((LAS v4i16_t*)p)); }
DI bf16x8 cat8(s16x4 a, s16x4 b) { return __builtin_shufflevector(a, b, 0, 1, 2, 3, 4, 5, 6, 7); }
template <int LI, int GI>
DI void glds16(unsigned voff, const void* sbase, unsigned ldsw) {
  asm volatile("s_add_u32 m0, %2, %3\n\ts_nop 0\n\tglobal_load_lds_dwordx4 %0, %1 offset:%4"
               :: "v"(voff), "s"(sbase), "s"(ldsw), "i"(LI - GI), "i"(GI) : "memory", "scc");
}
DI u32x4 mksrd(const void* p, unsigned bytes) { const unsigned long long a = (unsigned long long)p; return u32x4{(unsigned)a, (unsigned)(a >> 32) & 0xffffu, bytes, 0x00020000u}; }
template <int LI>
DI void bdma16(unsigned voff, u32x4 srd, unsigned soff, unsigned ldsw) {
  asm volatile("s_add_u32 m0, %3, %4\n\ts_nop 0\n\tbuffer_load_dwordx4 %0, %1, %2 offen lds"
               :: "v"(voff), "s"(srd), "s"(soff), "s"(ldsw), "i"(LI) : "memory", "scc");
}
template <int OFF>
DI void bload16_sc1(u32x4& dst, unsigned voff, u32x4 srd, unsigned soff) {
  asm volatile("buffer_load_dwordx4 %0, %1, %2, %3 offen offset:%4 sc1" : "=v"(dst) : "v"(voff), "s"(srd), "s"(soff), "i"(OFF) : "memory");
}
template <int OFF>
DI void gload16(bf16x8& dst, unsigned voff, const void* sbase) { asm volatile("global_load_dwordx4 %0, %1, %2 offset:%3" : "=v"(dst) : "v"(voff), "s"(sbase), "i"(OFF) : "memory"); }
template <int N> DI void waitv() { asm volatile("s_waitcnt vmcnt(%c0)" :: "i"(N) : "memory"); }
template <int OFF> DI void arrive(unsigned addr, unsigned one) { asm volatile("ds_add_u32 %0, %1 offset:%2" :: "v"(addr), "v"(one), "i"(OFF) : "memory"); }
template <int OFF> DI void check(unsigned v, unsigned addr, unsigned target) {
  asm volatile(
      "v_cmp_gt_u32 vcc, %2, %0\n\t"
      "s_cbranch_vccz 2f\n\t"
      "1:\n\t"
      "ds_read_b32 %0, %1 offset:%3\n\t"
      "s_waitcnt lgkmcnt(0)\n\t"
      "v_cmp_gt_u32 vcc, %2, %0\n\t"
      "s_cbranch_vccnz 1b\n\t"
      "2:"
      : "+v"(v) : "v"(addr), "s"(target), "i"(OFF) : "memory", "vcc");
}
constexpr int C_R = 0, C_L = 256, C_L0 = 512;
template <int LI> DI void set_m0(unsigned ldsw) { asm volatile("s_add_u32 m0, %0, %1" :: "s"(ldsw), "i"(LI) : "memory", "scc"); }
template <int OFF> DI void bdma_m0(unsigned voff, u32x4 srd, unsigned soff) {
  asm volatile("buffer_load_dwordx4 %0, %1, %2 offen offset:%3 lds" :: "v"(voff), "s"(srd), "s"(soff), "i"(OFF) : "memory");
}


template <int VM>
DI void swbar(unsigned addr, unsigned target) {
  unsigned v, s;
  asm volatile(
      "s_waitcnt vmcnt(%c4) lgkmcnt(0)\n\t"
      "s_mov_b64 exec, 1\n\t"
      "v_mov_b32 %0, 1\n\t"
      "ds_add_rtn_u32 %0, %2, %0\n\t"
      "s_waitcnt lgkmcnt(0)\n\t"
      "v_add_u32 %0, 1, %0\n\t"
      "1:\n\t"
      "s_nop 0\n\t"
      "v_readfirstlane_b32 %1, %0\n\t"
      "s_nop 1\n\t"
      "s_cmp_ge_u32 %1, %3\n\t"
      "s_cbranch_scc1 2f\n\t"
      "ds_read_b32 %0, %2\n\t"
      "s_waitcnt lgkmcnt(0)\n\t"
      "s_branch 1b\n\t"
      "2:\n\t"
      "s_mov_b64 exec, -1"
      : "=&v"(v), "=&s"(s) : "v"(addr), "s"(target), "i"(VM) : "memory", "scc");
}
DI void lds_wait(unsigned addr) {
  unsigned v, s;
  asm volatile(
      "1:\n\t"
      "ds_read_b32 %0, %2\n\t"
      "s_waitcnt lgkmcnt(0)\n\t"
      "v_readfirstlane_b32 %1, %0\n\t"
      "s_nop 1\n\t"
      "s_cmp_lg_u32 %1, 0\n\t"
      "s_cbranch_scc1 2f\n\t"
      "s_sleep 2\n\t"
      "s_branch 1b\n\t"
      "2:"
      : "=&v"(v), "=&s"(s) : "v"(addr) : "memory", "scc");
}
DI void lds_set(unsigned addr) {
  unsigned v;
  asm volatile("s_waitcnt lgkmcnt(0)\n\tv_mov_b32 %0, 1\n\tds_write_b32 %1, %0" : "=&v"(v) : "v"(addr) : "memory");
}
DI void gpoll(gu32* p, unsigned need) {
#pragma unroll 1
  for (unsigned i = 0; i < (1u << 18); ++i) {
    if (((unsigned)__ballot(__hip_atomic_load(p, RLX_AGENT) != 0u) & need) == need) break;
    __builtin_amdgcn_s_sleep(1);
  }
  __builtin_amdgcn_fence(__ATOMIC_ACQUIRE, "wavefront");
  asm volatile("" ::: "memory");
}
DI void gsignal(gu32* p, bool on) { if (on) __hip_atomic_store(p, 1u, RLX_AGENT); }

#define ST16(base, off, v) __builtin_amdgcn_raw_buffer_store_b128((v), __builtin_amdgcn_make_buffer_rsrc((void*)(base), 0, 1 << 20, 0x00020000), (off), 0, 16)
#define M16(a, b, c) __builtin_amdgcn_mfma_f32_16x16x32_bf16((a), (b), (c), 0, 0, 0)
DI float xg_sum(float v) {
  auto r = __builtin_amdgcn_permlane16_swap(__float_as_uint(v), __float_as_uint(v), false, false);
  v = __uint_as_float(r[0]) + __uint_as_float(r[1]);
  auto q = __builtin_amdgcn_permlane32_swap(__float_as_uint(v), __float_as_uint(v), false, false);
  return __uint_as_float(q[0]) + __uint_as_float(q[1]);
}
extern "C" __global__ __launch_bounds__(384) void k_ln_kv(const u16* __restrict__ x, const u16* __restrict__ wk, const u16* __restrict__ wv, const u16* __restrict__ wq,
                                               const u16* __restrict__ w1, const u16* __restrict__ b1, char* __restrict__ ws) {
  __shared__ __attribute__((aligned(16))) char img[6 * 9216];
  __shared__ __attribute__((aligned(16))) float stats[16 * 12];
  const int tid = threadIdx.x, lane = tid & 63, m = lane & 15, g = lane >> 4;
  const int w = __builtin_amdgcn_readfirstlane(tid >> 6);
  unsigned cpf;
  {
    const unsigned long long pc = __builtin_amdgcn_s_getpc() & ~255ull;
    unsigned csz;
    asm volatile("s_mov_b32 %0, k_main-k_ln_kv+22528" : "=s"(csz));
    unsigned co = (unsigned)(blockIdx.x >= 128 ? tid : (tid & 63)) * 128u;
    co = co < csz ? co : 0u;
    asm volatile("global_load_dword %0, %1, %2" : "=v"(cpf) : "v"(co), "s"(pc) : "memory");
  }
  if (blockIdx.x >= 128) {
    const int pb = blockIdx.x - 128, kq = pb & 3, hf = (pb >> 2) & 1, idx = pb >> 3;
    const int cg = kq * 4 + (idx & 3), r0 = (hf * 4 + (idx >> 2)) * 72;
    u32x4 t[9];
    unsigned dst[3];
#pragma unroll
    for (int r = 0; r < 3; ++r) {
      const int c = tid + r * 384, cc = c < 936 ? c : 935, rr = cc / 13, k = cc - rr * 13, row = r0 + rr;
      const int sw = (row >> 2) & 2;
      const u16* src = k < 5 ? wq + row * D + ((36 * cg) & ~7) + k * 8 : w1 + (size_t)row * F + 288 * cg + (k - 5) * 8;
      t[r] = *(const u32x4*)src;
      dst[r] = k < 4 ? row * 64 + (k ^ sw) * 16 : (k == 4 ? 36864 + row * 16 : 46080 + ((k - 5) >> 2) * 36864 + row * 64 + (((k - 5) & 3) ^ sw) * 16);
    }
#pragma unroll
    for (int r = 0; r < 6; ++r) { const int c = tid + r * 384, cc = c < 2016 ? c : 0, rr = cc / 28; t[3 + r] = *(const u32x4*)(w1 + (size_t)(r0 + rr) * F + 288 * cg + 64 + (cc - rr * 28) * 8); }
    const unsigned bpf = tid < 36 ? *(const unsigned*)(b1 + 288 * cg + tid * 8) : 0u;
#pragma unroll
    for (int r = 0; r < 3; ++r) ST16(ws + WS_IMG + (size_t)cg * IMG_CG, dst[r], t[r]);
#pragma unroll
    for (int r = 3; r < 9; ++r) asm volatile("" :: "v"(t[r]));
    asm volatile("" :: "v"(bpf));
    asm volatile("" :: "v"(cpf));
    return;
  }
  const int mt = blockIdx.x;
  const u16* xrow = x + (size_t)(16 * mt + m) * D;
  u32x4 xr[3];
#pragma unroll
  for (int i = 0; i < 3; ++i) xr[i] = *(const u32x4*)(xrow + 32 * (3 * w + i) + 8 * g);
  const float c0 = __uint_as_float((unsigned)xrow[0] << 16);
  typedef unsigned u32x4u __attribute__((ext_vector_type(4), aligned(4)));
  u32x4 ra[3], rb[3]; unsigned rc[3];
#pragma unroll
  for (int i = 0; i < 3; ++i) {
    const int r = lane + 64 * i;
    const char* src = (r < 96 ? (const char*)wk + r * 36 : (const char*)wv + (r - 96) * 36) + w * 3456;
    ra[i] = *(const u32x4u*)src; rb[i] = *(const u32x4u*)(src + 16); rc[i] = *(const unsigned*)(src + 32);
  }
  if (tid < 64) *(unsigned*)(ws + WS_FLAGS + mt * 256 + tid * 4) = 0u;
  if (mt == 127 && tid >= 64 && tid < 136) *(u32x4*)(ws + WS_KV + 64 * REC + (tid - 64) * 16) = u32x4{0u, 0u, 0u, 0u};
  char* im = img + w * 9216;
  float v[24], s = 0.f, q = 0.f;
#pragma unroll
  for (int i = 0; i < 3; ++i)
#pragma unroll
    for (int j = 0; j < 4; ++j) { v[8 * i + 2 * j] = bf_lo(xr[i][j]); v[8 * i + 2 * j + 1] = bf_hi(xr[i][j]); }
#pragma unroll
  for (int i = 0; i < 24; ++i) { const float d = v[i] - c0; s += d; q = __builtin_fmaf(d, d, q); }
  asm volatile("" : "+v"(s) : "v"(cpf));
  s = xg_sum(s); q = xg_sum(q);
  if (g == 0) *(f32x2_t*)&stats[m * 12 + 2 * w] = f32x2_t{s, q};
#pragma unroll
  for (int i = 0; i < 3; ++i) {
    char* dst = im + (lane + 64 * i) * 48;
    *(u32x4*)dst = ra[i]; *(u32x4*)(dst + 16) = rb[i]; *(unsigned*)(dst + 32) = rc[i];
  }
  const lds_ptr rd = (lds_ptr)im + (8 * g + (m >> 2)) * 48 + (m & 3) * 8, rd1 = (lds_ptr)im + (8 * g + (m >> 2)) * 48 + 32;
  bf16x8 kf[3][2], vf[3][2];
#pragma unroll
  for (int i = 0; i < 3; ++i) {
    kf[i][0] = cat8(trread(rd + i * 1536), trread(rd + i * 1536 + 192));
    kf[i][1] = cat8(trread(rd1 + i * 1536), trread(rd1 + i * 1536 + 192));
    vf[i][0] = cat8(trread(rd + 4608 + i * 1536), trread(rd + 4608 + i * 1536 + 192));
    vf[i][1] = cat8(trread(rd1 + 4608 + i * 1536), trread(rd1 + 4608 + i * 1536 + 192));
  }
  __syncthreads();
  s = 0.f; q = 0.f;
#pragma unroll
  for (int i = 0; i < 3; ++i) { const f32x4 t = *(const f32x4*)&stats[m * 12 + 4 * i]; s += t[0] + t[2]; q += t[1] + t[3]; }
  const float md = s * (1.f / 576.f);
  const float var = __builtin_fmaxf(__builtin_fmaf(-md, md, q * (1.f / 576.f)), 0.f);
  const float mb = bfr(c0 + md), sd = bfr(__builtin_amdgcn_sqrtf(bfr(bfr(var) + 1e-5f)));
  const float rinv = __builtin_amdgcn_rcpf(sd);
  f32x4 acc[4] = {};
#pragma unroll
  for (int i = 0; i < 3; ++i) {
    u32x4 o;
#pragma unroll
    for (int j = 0; j < 4; ++j) {
      const unsigned dp = cvtpk(v[8 * i + 2 * j] - mb, v[8 * i + 2 * j + 1] - mb);
      o[j] = cvtpk(bf_lo(dp) * rinv, bf_hi(dp) * rinv);
    }
    ST16(ws + WS_XNF + ((size_t)mt * 18 + 3 * w) * 1024, (unsigned)(i * 1024 + lane * 16), o);
    acc[0] = M16(kf[i][0], BC(o), acc[0]);
    acc[1] = M16(kf[i][1], BC(o), acc[1]);
    acc[2] = M16(BC(o), vf[i][0], acc[2]);
    acc[3] = M16(BC(o), vf[i][1], acc[3]);
  }
#pragma unroll
  for (int i = 0; i < 4; ++i) *(f32x4*)(im + i * 1024 + lane * 16) = acc[i];
  __syncthreads();
  if (w < 4) {
    f32x4 a = *(const f32x4*)(img + w * 1024 + lane * 16);
#pragma unroll
    for (int ww = 1; ww < 6; ++ww) a += *(const f32x4*)(img + ww * 9216 + w * 1024 + lane * 16);
    u32x2 p = {cvtpk(a[0], a[1]), cvtpk(a[2], a[3])};
    char* rec = ws + WS_KV + (size_t)(mt >> 1) * REC;
    const int key = 16 * (mt & 1) + m;
    if (w == 0) *(u32x2*)(rec + (key + 32 * (g & 1)) * 16 + (g >> 1) * 8) = p;
    else if (w == 1) { if (g == 0) *(unsigned*)(rec + 1024 + key * 4) = p[0]; }
    else {
      const int dc = (w - 2) * 16 + m;
      if (dc == DH) p = u32x2{0x3f803f80u, 0x3f803f80u};
      if (dc <= DH) *(u32x2*)(rec + REC + 1152 + ((mt & 1) * 2 + (g & 1)) * 304 + dc * 16 + (g >> 1) * 8) = p;
    }
  }
}

constexpr int M_OFF = 90;
constexpr float L_TRIG = 9.5367431640625e-07f;
constexpr float L_MAX = 1.3292280e36f;
constexpr float L_MIN = 1e-29f;
constexpr float M_MAX = 4194304.f;

DI void set_negm(u32x2& q1, float m, int h) {
  const float nm = -m;
  const unsigned hi = __float_as_uint(nm) & 0xffff0000u;
  const float r1 = nm - __uint_as_float(hi);
  const unsigned mid = __float_as_uint(r1) & 0xffff0000u;
  const float r2 = r1 - __uint_as_float(mid);
  q1[1] = h ? (__float_as_uint(r2) >> 16) : ((hi >> 16) | mid);
}
DI float half_max(float v) {
  auto rr = __builtin_amdgcn_permlane32_swap(__float_as_uint(v), __float_as_uint(v), false, false);
  return __builtin_fmaxf(__uint_as_float(rr[0]), __uint_as_float(rr[1]));
}
DI float tile_max(const f32x16& s) {
  float a = __builtin_fmaxf(s[0], s[1]);
#pragma unroll
  for (int r = 2; r < 16; ++r) a = __builtin_fmaxf(a, s[r]);
  return half_max(a);
}
#define MFMA8(a, b) __builtin_amdgcn_mfma_f32_32x32x8bf16_1k(__builtin_bit_cast(s16x4, a), __builtin_bit_cast(s16x4, b), f32x16{}, 0, 0, 0)
DI f32x16 attn_slow(const char* kv, bf16x8 q0, u32x2 q1, int lane) {
  const int m = lane & 31, h = lane >> 5, vr = m < DH ? m : DH;
  q1[1] = 0u;
  float mx = -__builtin_inff();
  f32x16 o = f32x16{};
#pragma unroll 1
  for (int t = 0; t < 64; ++t) {
    const char* rk = kv + (size_t)t * REC; const char* rv = rk + REC + 1152 + h * 304 + vr * 16;
    const bf16x8 k0 = *(const bf16x8*)(rk + lane * 16);
    const u32x2 k1 = {*(const unsigned*)(rk + 1024 + m * 4), 0u};
    const bf16x8 v0 = *(const bf16x8*)rv, v1 = *(const bf16x8*)(rv + 608);
    f32x16 s = MFMA8(k1, q1);
    s = MFMA(k0, q0, s);
    const float mn = __builtin_fmaxf(mx, tile_max(s));
    const float al = __builtin_amdgcn_exp2f(mx - mn);
    mx = mn;
    u32x4 p0, p1;
#pragma unroll
    for (int j = 0; j < 4; ++j) {
      p0[j] = cvtpk(__builtin_amdgcn_exp2f(s[2 * j] - mn), __builtin_amdgcn_exp2f(s[2 * j + 1] - mn));
      p1[j] = cvtpk(__builtin_amdgcn_exp2f(s[8 + 2 * j] - mn), __builtin_amdgcn_exp2f(s[8 + 2 * j + 1] - mn));
    }
#pragma unroll
    for (int r = 0; r < 16; ++r) o[r] *= al;
    o = MFMA(v0, BC(p0), o);
    o = MFMA(v1, BC(p1), o);
  }
  return o;
}
DI unsigned relu2pk(float a, float b) {
  unsigned r; float t;
  asm volatile("v_mul_f32_e64 %0, %2, |%2|\n\tv_mul_f32_e64 %1, %3, |%3|\n\tv_cvt_pk_bf16_f32 %0, %0, %1\n\tv_pk_max_i16 %0, %0, 0" : "=&v"(r), "=&v"(t) : "v"(a), "v"(b));
  return r;
}
DI void mfma_dc(f32x4& d, bf16x8 a, bf16x8 b, const f32x4& c) { asm volatile("v_mfma_f32_16x16x32_bf16 %0, %1, %2, %3" : "=&v"(d) : "v"(a), "v"(b), "v"(c)); }
DI void mfma_acc(f32x4& c, bf16x8 a, bf16x8 b) { asm volatile("v_mfma_f32_16x16x32_bf16 %0, %1, %2, %0" : "+v"(c) : "v"(a), "v"(b)); }
DI void mfma_z(f32x4& d, bf16x8 a, bf16x8 b) { asm volatile("v_mfma_f32_16x16x32_bf16 %0, %1, %2, 0" : "=&v"(d) : "v"(a), "v"(b)); }
template <int OFF>
DI void bload8_sc1(u32x2& dst, unsigned voff, u32x4 srd) { asm volatile("buffer_load_dwordx2 %0, %1, %2, 0 offen offset:%3 sc1" : "=v"(dst) : "v"(voff), "s"(srd), "i"(OFF) : "memory"); }

template <int N, class Fn> DI void static_for(Fn&& f) {
  if constexpr (N > 0) { static_for<N - 1>(f); f(std::integral_constant<int, N - 1>{}); }
}

extern "C" __global__ __launch_bounds__(512, 2) void k_main(const u16* __restrict__ x, const u16* __restrict__ wo, const u16* __restrict__ w1,
                                                 const u16* __restrict__ b1, const u16* __restrict__ w2, const u16* __restrict__ b2, char* __restrict__ ws, u16* __restrict__ out) {
  extern __shared__ __attribute__((aligned(16))) char smem[];
  const int xcd = blockIdx.x & 7, bidx = blockIdx.x >> 3;
  const int kq = xcd & 3, nq = bidx & 3, cg = kq * 4 + nq, rb = (xcd >> 2) * 8 + (bidx >> 2);
  const int tid = threadIdx.x, lane = tid & 63, m = lane & 31, h = lane >> 5;
  const int w8 = __builtin_amdgcn_readfirstlane(tid >> 6), w = w8 & 3;
  const int mt = rb * 4 + w;
  const unsigned lds0 = (unsigned)(uintptr_t)(lds_ptr)smem;
  const unsigned voff = lane * 16;
  const int n0q = (36 * cg) & ~7;
#define MKRS(off, bytes) __builtin_amdgcn_make_buffer_rsrc((void*)(ws + (off)), 0, (int)(bytes), 0x00020000)
  gu32* flags = (gu32*)(ws + WS_FLAGS);

  if (w8 < 4) {
    const int prow = lane >> 2, pch = (lane & 3) ^ ((lane >> 5) << 1);
    const int g4 = lane >> 4, i16 = lane & 15;
    const unsigned ldsw = lds0 + L_G + 9 * w * 1024;
    bf16x8 xf[36];
    const char* xb = ws + WS_XNF + (size_t)mt * 36 * 1024;
    auto xload = [&](auto uc) { constexpr int u = uc, o = ((u & 1) * 18 + (u >> 1)) * 1024; gload16<(o & 4095)>(xf[u], voff, xb + (o & ~4095)); };
    static_for<36>([&](auto u) { xload(u); });
    u32x4 srdW = mksrd(w1, D * F * 2);
    unsigned so[9];
#pragma unroll
    for (int p = 0; p < 9; ++p) so[p] = (unsigned)((9 * w + p) * 16 * F * 2 - (p & 3) * 1024);
    unsigned voffA = (unsigned)((prow * F + pch * 8 + 288 * cg) * 2) + 64u, voffO = voffA, voffB = voffA;
    PIN(voffO); PIN(voffB);
    unsigned aC = lds0 + L_CNT + lane * 4, one = 1u, tgt = 4u;
    PIN(aC); PIN(one);
    lds_ptr pC = (lds_ptr)smem + L_CNT + lane * 4; PIN(pC);
    const int rdo = (8 * g4 + (i16 >> 2)) * 64 + (g4 & 1) * 32 + (i16 & 3) * 8;
    const lds_ptr rd0 = (lds_ptr)smem + L_G + rdo, rd1 = (lds_ptr)smem + L_G + (rdo ^ 32);
    lds_ptr biasp = (lds_ptr)smem + L_BIAS + g4 * 16;
    asm volatile("s_barrier" ::: "memory");
    __builtin_amdgcn_s_setprio(2);
    s16x4 wr[18];
    f32x4 A[4], P[4], Bi[2];
    {
      const lds_ptr rq1 = (lds_ptr)smem + L_Q1 + (8 * g4 + (i16 >> 2)) * 16 + (i16 & 1) * 8;
      auto qread = [&](auto itc) {
        constexpr int it = itc, st = it / 6, tl = (it % 6) >> 1, hf = it & 1;
        if constexpr (st < 18) {
          if constexpr (tl == 0) wr[it % 12] = trread(rd0 + st * 2048 + hf * 256);
          else if constexpr (tl == 1) wr[it % 12] = trread(rd1 + st * 2048 + hf * 256);
          else wr[it % 12] = trread(rq1 + st * 512 + hf * 64);
        }
      };
      static_for<11>([&](auto it) { qread(it); });
      static_for<18>([&](auto ksc) {
        constexpr int ks = ksc;
        waitv<34 - 2 * ks>();
        static_for<6>([&](auto sc) {
          constexpr int sI = sc, tl = sI >> 1, mi = sI & 1, gi = 6 * ks + sI;
          const bf16x8 wf = cat8(wr[(6 * ks + 2 * tl) % 12], wr[(6 * ks + 2 * tl + 1) % 12]);
          f32x4& Dd = tl < 2 ? A[sI & 3] : P[mi];
          if constexpr (ks == 0) mfma_z(Dd, wf, xf[2 * ks + mi]); else mfma_acc(Dd, wf, xf[2 * ks + mi]);
          SBAR();
          qread(std::integral_constant<int, gi + 11>{});
          if constexpr (gi + 11 == 107) arrive<C_R>(aC, one);
          SBAR();
        });
      });
      asm volatile("s_nop 7" : "+v"(A[0]), "+v"(A[1]), "+v"(A[2]), "+v"(A[3]), "+v"(P[0]), "+v"(P[1]));
      const lds_ptr qw = (lds_ptr)smem + L_Q + (32 * w + i16) * QROW + g4 * 8;
#pragma unroll
      for (int i = 0; i < 4; ++i) *(LAS u32x2*)(qw + (i & 1) * 16 * QROW + (i >> 1) * 32) = u32x2{cvtpk(A[i][0] * C2, A[i][1] * C2), cvtpk(A[i][2] * C2, A[i][3] * C2)};
      if (g4 < 2) {
#pragma unroll
        for (int i = 0; i < 2; ++i) *(LAS u32x2*)(qw + i * 16 * QROW + 64) = u32x2{cvtpk(P[i][0] * C2, P[i][1] * C2), cvtpk(P[i][2] * C2, P[i][3] * C2)};
      }
      lds_set(lds0 + L_SYNC + SY_Q + w * 4);
      P[2] = P[0]; P[3] = P[1];
      check<C_L0>(0u, aC, 4u);
    }
    const unsigned vA2 = (unsigned)((((lane >> 2) & 3) * D + (lane >> 4) * 32 + (lane & 3) * 8 + 144 * nq) * 2);
    const unsigned vO2 = (unsigned)((((lane >> 2) & 3) * D + (lane >> 4) * 32 + ((lane & 3) ^ 2) * 8 + 144 * nq) * 2);
    unsigned vB2 = (unsigned)(((lane >> 1) * D + 128 + (lane & 1) * 8 + 144 * nq) * 2);
    auto kpos = [&](int i) { int u = (i < 2 ? i + 7 : (i < 9 ? i - 2 : i)) + 9 * nq; return u >= 36 ? u - 36 : u; };
    const unsigned tabD = lane < 9 ? (unsigned)((1152 * kq + 32 * kpos(4 * lane + w)) * D * 2) : (unsigned)((144 * kq + 36 * w) * D * 2);
    const unsigned tabH = lane < 36 ? (unsigned)kpos(lane) * 2048u : (unsigned)(lane & 3) * 2048u;
    auto set_g2 = [&](int j) {
      const unsigned r = __builtin_amdgcn_readlane(tabD, j);
      voffA = vA2 + r; voffO = vO2 + r; voffB = vB2 + r;
    };
    auto bias_read = [&]() { Bi[0] = *(const LAS f32x4*)biasp; Bi[1] = *(const LAS f32x4*)(biasp + 64); biasp += 128; };
    const __amdgpu_buffer_rsrc_t rsh = MKRS(WS_HF + (unsigned)(mt * 288 + 18 * cg) * 1024, 18 * 1024);
    unsigned hst = voff + 16384u;
    unsigned ep[8] = {}, linc = 0u;
    int sl_cur = GSLOT, sl_dma = 0;
    lds_ptr ra = rd0 + GSLOT, rb1 = rd1 + GSLOT;
    auto wread1 = [&](auto itc) { constexpr int it = itc, st = (it >> 2) % 18; wr[it % 12] = trread(((it & 2) ? rb1 : ra) + st * 2048 + (it & 1) * 256); };
    auto dma_piece = [&](auto pc) { constexpr int p = pc; bdma_m0<(p & 3) * 1024>(p == 8 ? voffB : ((p & 1) ? voffO : voffA), srdW, so[p]); };
    auto pin_so = [&]() {
#pragma unroll
      for (int p = 0; p < 9; ++p) PINS(so[p]);
    };
    bias_read();
    static_for<11>([&](auto it) { wread1(it); });
#pragma unroll 1
    for (int t = 0; t < 9; ++t) {
      voffA += 64; voffO += 64; voffB += 64;
      if (__builtin_expect(t >= 7, 0)) {
        srdW = mksrd(w2, F * D * 2);
#pragma unroll
        for (int p = 0; p < 9; ++p) so[p] = p < 8 ? (unsigned)(p * 4 * D * 2 - (p & 3) * 1024) : 0u;
        set_g2(t - 7);
        __builtin_amdgcn_s_setprio(0);
      }
      pin_so();
      const unsigned ldsd = ldsw + sl_dma;
      const int sl_nxt = sl_cur == 2 * GSLOT ? 0 : sl_cur + GSLOT;
      unsigned cv;
      static_for<72>([&](auto gc) {
        constexpr int gi = gc, ks = gi >> 2, tl = (gi >> 1) & 1, mi = gi & 1;
        if constexpr ((gi & 3) == 0) asm volatile("" :: "v"(wr[gi % 12 + 3]));
        const bf16x8 wf = cat8(wr[(4 * ks + 2 * tl) % 12], wr[(4 * ks + 2 * tl + 1) % 12]);
        if constexpr (ks == 0) mfma_dc(A[gi & 3], wf, xf[2 * ks + mi], Bi[tl]);
        else if constexpr (ks == 17) mfma_dc(P[gi & 3], wf, xf[2 * ks + mi], A[gi & 3]);
        else mfma_acc(A[gi & 3], wf, xf[2 * ks + mi]);
        SBAR();
        if constexpr (gi == 11) { check<C_R>(cv, aC, tgt); tgt += 4u; }
        if constexpr (gi == 61) { check<C_L>(cv, aC, tgt); ra = rd0 + sl_nxt; rb1 = rd1 + sl_nxt; }
        if constexpr (gi == 12 || gi == 40 || gi == 68) set_m0<(gi - 12) / 28 * 4096>(ldsd);
        wread1(std::integral_constant<int, gi + 11>{});
        if constexpr (gi == 0) cv = *(volatile LAS unsigned*)(pC + C_R);
        if constexpr (gi == 52) cv = *(volatile LAS unsigned*)(pC + C_L);
        if constexpr (gi == 60) arrive<C_R>(aC, one);
        if constexpr (gi >= 12 && (gi - 12) % 7 == 0) dma_piece(std::integral_constant<int, (gi - 12) / 7>{});
        if constexpr (gi >= 6 && gi <= 34 && gi % 4 == 2) {
          constexpr int j = (gi - 6) / 4, pi = ((j >> 1) & 1) * 2 + (j >> 2);
          ep[j] = relu2pk(P[pi][2 * (j & 1)], P[pi][2 * (j & 1) + 1]);
        }
        if constexpr (gi == 23 || gi == 37) {
          constexpr int hh = gi == 37;
          __builtin_amdgcn_raw_buffer_store_b128(u32x4{ep[4 * hh], ep[4 * hh + 1], ep[4 * hh + 2], ep[4 * hh + 3]}, rsh, hst + hh * 1024, 0, 16);
        }
        if constexpr (gi == 46) { waitv<7>(); arrive<C_L>(aC, linc); linc = 1u; PIN(linc); hst = voff + (unsigned)t * 2048u; PIN(hst); }
        if constexpr (gi == 44) bias_read();
        SBAR();
      });
      sl_dma = sl_cur; sl_cur = sl_nxt;
    }
    u32x4 hf[8];
    u32x4 srdH = mksrd(ws + WS_HF + ((size_t)mt * 288 + 72 * kq) * 1024, 72 * 1024);
    u32x4 srdX = mksrd(ws + WS_OX + (size_t)(mt * 16 + 4 * kq) * 256, 0);
    unsigned sh[4];
    auto set_h = [&](int j) {
#pragma unroll
      for (int i = 0; i < 4; ++i) sh[i] = __builtin_amdgcn_readlane(tabH, 4 * j + i);
    };
    set_h(0);
    asm volatile("s_nop 4" : "+s"(sh[2]), "+s"(sh[3]));
    static_for<4>([&](auto i) { bload16_sc1<(i & 1) * 1024>(hf[4 + i], voff, srdH, sh[2 + (i >> 1)]); });
    hf[0] = u32x4{ep[0], ep[1], ep[2], ep[3]}; hf[1] = u32x4{ep[4], ep[5], ep[6], ep[7]};
    const int r2o = g4 * 1024 + (i16 >> 2) * 64 + (g4 & 1) * 32 + (i16 & 3) * 8;
    const lds_ptr r2a0 = (lds_ptr)smem + L_G + r2o, r2b0 = (lds_ptr)smem + L_G + (r2o ^ 32);
    const lds_ptr r2h0 = (lds_ptr)smem + L_G + 8192 + (4 * g4 + (i16 >> 2)) * 32 + (i16 & 3) * 8;
    lds_ptr r2a = r2a0 + sl_cur, r2b = r2b0 + sl_cur, r2h = r2h0 + sl_cur;
    auto wread2 = [&](auto itc) {
      constexpr int it = itc, ks = (it / 18) & 3, nt = (it % 18) >> 1, hi = it & 1;
      if constexpr (nt < 8) wr[it % 18] = trread(((nt & 1) ? r2b : r2a) + ks * 9216 + hi * 4096 + (nt >> 1) * 256);
      else wr[it % 18] = trread(r2h + ks * 9216 + hi * 512);
    };
    static_for<17>([&](auto it) { wread2(it); });
    asm volatile("s_nop 7" : "+v"(P[0]), "+v"(P[1]), "+v"(P[2]), "+v"(P[3]));
#pragma unroll
    for (int j = 0; j < 8; ++j) { const int pi = ((j >> 1) & 1) * 2 + (j >> 2); ep[j] = relu2pk(P[pi][2 * (j & 1)], P[pi][2 * (j & 1) + 1]); }
    hf[2] = u32x4{ep[0], ep[1], ep[2], ep[3]}; hf[3] = u32x4{ep[4], ep[5], ep[6], ep[7]};
    __builtin_amdgcn_raw_buffer_store_b128(hf[2], rsh, hst, 0, 16);
    __builtin_amdgcn_raw_buffer_store_b128(hf[3], rsh, hst + 1024, 0, 16);
    f32x4 acc[18] = {};
    const unsigned voffX = (unsigned)(g4 * 256 + i16 * 8);
    gu32* fline = flags + (mt * 4 + kq) * 32;
    const unsigned foff = (unsigned)i16 * 4u;
    unsigned fl = 0u;
    const unsigned tabN = lane == 8 ? (0xf0u | (0x100u << nq)) : (((lane & 1) && lane < 6) ? 1u << ((nq + ((lane + 1) >> 1)) & 3) : 0u);
    u32x2 ox0, ox1;
#pragma unroll 1
    for (int j = 0; j < 10; ++j) {
      const int sl_nxt = sl_cur == 2 * GSLOT ? 0 : sl_cur + GSLOT;
      int nD = sl_nxt, nH = sl_nxt;
      unsigned need = __builtin_amdgcn_readlane(tabN, j);
      if (__builtin_expect(j >= 7, 0)) {
        if (j == 7) {
          srdW = mksrd(wo, D * D * 2);
#pragma unroll
          for (int p = 4; p < 8; ++p) so[p] += 2 * D * 2;
          vB2 += (unsigned)(h * 2 * D * 2);
        } else if (j == 8) {
          srdW[2] = 0u;
          srdH = mksrd(ws + WS_OF + ((size_t)mt * 32 + 8 * kq) * 1024, 8 * 1024);
        } else {
          srdH[2] = 0u; srdX[2] = 1024u;
          nD = L_EX - L_G; nH = L_EX + 4096 - L_G - 8192;
        }
      }
      pin_so();
      set_g2(j + 2); set_h(j + 1);
      const unsigned ldsd = ldsw + sl_dma;
      unsigned cv;
      static_for<72>([&](auto gc) {
        constexpr int gi = gc, ks = gi / 18, nt = (gi % 18) >> 1, mi = gi & 1;
        if constexpr (gi % 18 == 0) { waitv<(ks == 0 ? 14 : (ks == 1 ? 15 : (ks == 2 ? 13 : 16)))>(); if constexpr (ks == 2) arrive<C_L>(aC, one); }
        if constexpr (gi % 6 == 0) asm volatile("" :: "v"(wr[gi % 18 + 5]));
        const bf16x8 wf = cat8(wr[2 * nt], wr[2 * nt + 1]);
        mfma_acc(acc[gi % 18], wf, BC(hf[2 * ks + mi]));
        SBAR();
        if constexpr (gi == 5) { check<C_R>(cv, aC, tgt); tgt += 4u; }
        if constexpr (gi == 55) { check<C_L>(cv, aC, tgt); r2a = r2a0 + nD; r2b = r2b0 + nD; r2h = r2h0 + nH; }
        constexpr int dp = gi == 6 ? 0 : gi == 11 ? 1 : gi == 21 ? 2 : gi == 26 ? 3 : gi == 31 ? 4 : gi == 40 ? 5 : gi == 46 ? 6 : gi == 58 ? 7 : gi == 64 ? 8 : -1;
        if constexpr (dp >= 0 && (dp & 3) == 0) set_m0<dp * 1024>(ldsd);
        wread2(std::integral_constant<int, gi + 17>{});
        if constexpr (gi == 0) cv = *(volatile LAS unsigned*)(pC + C_R);
        if constexpr (gi == 44) cv = *(volatile LAS unsigned*)(pC + C_L);
        if constexpr (gi == 54) arrive<C_R>(aC, one);
        if constexpr (gi == 2) bload8_sc1<0>(ox0, voffX, srdX);
        if constexpr (gi == 3) bload8_sc1<128>(ox1, voffX, srdX);
        if constexpr (dp >= 0) dma_piece(std::integral_constant<int, (dp >= 0 ? dp : 0)>{});
        if constexpr (gi == 15) {
          if (need) {
            waitv<10>(); PIN(fl);
            if (((unsigned)__ballot(fl != 0u) & need) != need) { gpoll(fline + i16, need); }
          }
        }
        if constexpr (gi == 49) asm volatile("global_load_dword %0, %1, %2 sc1" : "+v"(fl) : "v"(foff), "s"(fline) : "memory");
        if constexpr (gi % 18 >= 16) bload16_sc1<mi * 1024>(hf[2 * ks + mi], voff, srdH, sh[ks]);
        if constexpr (gi == 24) { if (j == 0) { waitv<7>(); gsignal(fline + nq, lane == 0); } }
        SBAR();
      });
      sl_dma = sl_cur; sl_cur = sl_nxt;
    }
    waitv<0>();
    asm volatile("s_nop 7" : "+v"(ox0), "+v"(ox1) : "v"(fl), "v"(hf[0]), "v"(hf[1]), "v"(hf[2]), "v"(hf[3]), "v"(hf[4]), "v"(hf[5]), "v"(hf[6]), "v"(hf[7]));
#pragma unroll
    for (int i = 0; i < 18; ++i) acc[i] = __builtin_amdgcn_mfma_f32_16x16x16bf16_1k(wr[i & ~1], __builtin_bit_cast(s16x4, (i & 1) ? ox1 : ox0), acc[i], 0, 0, 0);
    if (w != kq) {
      const __amdgpu_buffer_rsrc_t rsp = MKRS(WS_PART + (unsigned)((mt * 4 + nq) * 4 + kq) * 9 * 1024, 9 * 1024);
#pragma unroll
      for (int nt = 0; nt < 9; ++nt) {
        auto r0 = __builtin_amdgcn_permlane16_swap(cvtpk(acc[2 * nt][0], acc[2 * nt][1]), cvtpk(acc[2 * nt + 1][0], acc[2 * nt + 1][1]), false, false);
        auto r1 = __builtin_amdgcn_permlane16_swap(cvtpk(acc[2 * nt][2], acc[2 * nt][3]), cvtpk(acc[2 * nt + 1][2], acc[2 * nt + 1][3]), false, false);
        __builtin_amdgcn_raw_buffer_store_b128(u32x4{r0[0], r1[0], r0[1], r1[1]}, rsp, voff + nt * 1024, 0, 16);
      }
    } else {
#pragma unroll
      for (int gi = 0; gi < 18; ++gi) *(LAS f32x4*)((lds_ptr)smem + L_KV + gi * 1024 + voff) = acc[gi];
      lds_set(lds0 + L_SYNC + SY_F);
    }
  } else {
    __builtin_amdgcn_s_setprio(1);
    const char* kv = ws + WS_KV;
    const unsigned ldsk = lds0 + L_KV + 5 * w * 1024;
    const char* kvsrc = kv + REC + 5 * w * 1024;
    const int t4 = tid & 255;
    const unsigned bv0 = b1[288 * cg + t4], bv1 = b1[288 * cg + 256 + (t4 & 31)];
    {
      const u32x4 srdI = mksrd(ws + WS_IMG + (size_t)cg * IMG_CG, IMG_CG);
      const unsigned ldsw = lds0 + L_G + 9 * w * 1024, si = (unsigned)(9 * w * 1024);
      static_for<9>([&](auto p) { bdma16<p * 1024>(voff, srdI, si + p * 1024, ldsw); });
      bdma16<L_Q1>(voff, srdI, 36864u + w * 1024, lds0 + w * 1024);
      bdma16<L_Q1 + 4096>(voff, srdI, 36864u + 4096 + w * 1024, lds0 + w * 1024);
      bdma16<L_Q1 + 8192>(voff, srdI, 36864u + 8192, lds0);
      if (t4 < 16) *(unsigned*)(smem + L_SYNC + t4 * 4) = 0u;
      if (t4 < 192) *(unsigned*)(smem + L_CNT + t4 * 4) = 0u;
      asm volatile("s_waitcnt vmcnt(0) lgkmcnt(0)\n\ts_barrier" ::: "memory");
      float* biasT = (float*)(smem + L_BIAS);
      biasT[t4] = __uint_as_float(bv0 << 16);
      biasT[256 + (t4 & 31)] = __uint_as_float(bv1 << 16);
      __builtin_amdgcn_s_sleep(14);
      static_for<9>([&](auto p) { bdma16<GSLOT + p * 1024>(voff, srdI, 46080u + si + p * 1024, ldsw); });
      static_for<4>([&](auto p) { glds16<p * 1024, p * 1024>(voff, kvsrc, ldsk); });
      glds16<4096, 0>(voff, kvsrc + 4096, ldsk);
      static_for<9>([&](auto p) { bdma16<2 * GSLOT + p * 1024>(voff, srdI, 46080u + 36864u + si + p * 1024, ldsw); });
    }
    bf16x8 kA0 = *(const bf16x8*)(kv + lane * 16), kB0;
    u32x2 kA1 = {*(const unsigned*)(kv + 1024 + m * 4), 0x3f803f80u}, kB1 = {0u, 0x3f803f80u};
    waitv<16>();
    const unsigned aC = lds0 + L_CNT + lane * 4;
    arrive<C_L0>(aC, 1u);
    lds_wait(lds0 + L_SYNC + SY_Q + w * 4);
    const LAS unsigned* qrow = (const LAS unsigned*)((lds_ptr)smem + L_Q + (32 * w + m) * QROW + (36 * cg - n0q) * 2);
    u32x4 qa0 = {qrow[2 * h], qrow[2 * h + 1], qrow[4 + 2 * h], qrow[5 + 2 * h]};
    u32x4 qb0 = {qrow[9 + 2 * h], qrow[10 + 2 * h], qrow[13 + 2 * h], qrow[14 + 2 * h]};
    u32x2 qa1 = {qrow[8], 0u}, qb1 = {qrow[17], 0u};
    qa1[0] = h ? 0u : qa1[0]; qb1[0] = h ? 0u : qb1[0];
    bf16x8 vA0 = {}, vA1 = {}, vB0, vB1;
    f32x16 sa = MFMA8(kA1, qa1); sa = MFMA(kA0, BC(qa0), sa);
    f32x16 sb = MFMA8(kA1, qb1); sb = MFMA(kA0, BC(qb0), sb);
    float ma = __builtin_rintf(tile_max(sa)) + (float)M_OFF, mb = __builtin_rintf(tile_max(sb)) + (float)M_OFF;
    const bool bad0 = !(ma > -M_MAX) || !(mb > -M_MAX);
    set_negm(qa1, ma, h); set_negm(qb1, mb, h);
#pragma unroll
    for (int r = 0; r < 16; ++r) sa[r] -= ma;
    f32x16 oa = {}, ob = {};
    u32x4 pa0, pa1, pb0 = {}, pb1 = {};
    PIN(kA1); PIN(kB1);
    const int vr = m < DH ? m : DH;
    lds_ptr kp0 = (lds_ptr)smem + L_KV + lane * 16, kp1 = (lds_ptr)smem + L_KV + 1024 + m * 4, vp = (lds_ptr)smem + L_KV + 1152 + h * 304 + vr * 16;
    unsigned abar_addr = lds0 + L_SYNC + SY_A; PIN(abar_addr);
    unsigned pfj = 0u;
    const __amdgpu_buffer_rsrc_t rsp = MKRS(WS_PART + (unsigned)(((rb * 4 + kq) * 4 + nq) * 4) * 9 * 1024, 4 * 9 * 1024);
#define EX(v) __builtin_amdgcn_exp2f(v)
#define GAP(MF, SS, PW, i) do { MF; SBAR(); { const float e0 = EX(SS[4 * (i)]), e1 = EX(SS[4 * (i) + 1]), e2 = EX(SS[4 * (i) + 2]), e3 = EX(SS[4 * (i) + 3]); \
      PW[(2 * (i)) & 3] = cvtpk(e0, e1); PW[(2 * (i) + 1) & 3] = cvtpk(e2, e3); } PIN(PW); SBAR(); } while (0)
#define RESC(FL, O, SS, M, Q1) do { if (__builtin_expect(FL, 0)) { \
      int e = __builtin_amdgcn_frexp_expf(O[10]) + M_OFF; e = e > 0 ? e : 0; const float ef = (float)e; \
      _Pragma("unroll") for (int r = 0; r < 16; ++r) { O[r] = __builtin_amdgcn_ldexpf(O[r], -e); SS[r] -= ef; } \
      M += ef; set_negm(Q1, M, h); } SBAR(); } while (0)
#define ITER(tt, CK, KC0, KC1, VC0, VC1, KN0, KN1, VN0, VN1) do { \
    bool flb = false, fla = false; \
    GAP(sb = MFMA8(KC1, qb1); SBAR(); KN1[0] = *(const LAS unsigned*)(kp1 + (tt) * REC); KN0 = *(const LAS bf16x8*)(kp0 + (tt) * REC); if (CK) flb = __any(!(ob[10] <= L_TRIG)), sa, pa0, 0); \
    GAP(sb = MFMA(KC0, BC(qb0), sb); SBAR(); VN0 = *(const LAS bf16x8*)(vp + (tt) * REC); VN1 = *(const LAS bf16x8*)(vp + (tt) * REC + 608), sa, pa0, 1); \
    GAP(ob = MFMA(VC0, BC(pb0), ob), sa, pa1, 2); \
    GAP(ob = MFMA(VC1, BC(pb1), ob), sa, pa1, 3); \
    if (CK) RESC(flb, ob, sb, mb, qb1); \
    asm volatile("" :: "v"(VN1)); \
    GAP(sa = MFMA8(KN1, qa1); SBAR(); if (CK) fla = __any(!(oa[10] <= L_TRIG)), sb, pb0, 0); \
    GAP(sa = MFMA(KN0, BC(qa0), sa), sb, pb0, 1); \
    GAP(oa = MFMA(VN0, BC(pa0), oa), sb, pb1, 2); \
    GAP(oa = MFMA(VN1, BC(pa1), oa), sb, pb1, 3); \
    if (CK) RESC(fla, oa, sa, ma, qa1); \
  } while (0)
#pragma unroll 1
    for (int c = 0; c < 8; ++c) {
      {
        if (c >= 4 && c <= 6 && w != kq) swbar<3>(abar_addr, 4 * (c + 1)); else swbar<0>(abar_addr, 4 * (c + 1));
        if (c < 7) {
          const char* src = kvsrc + (size_t)(c + 1) * CHUNK; const unsigned dst = ldsk + ((c + 1) & 1) * CHUNK;
          static_for<4>([&](auto p) { glds16<p * 1024, p * 1024>(voff, src, dst); });
          glds16<4096, 0>(voff, src + 4096, dst);
        }
        if (c < 3) {
          const u32x4 s2 = mksrd(w2 + (size_t)1152 * kq * D, 1152 * D * 2);
          const unsigned li = lane < 27 ? (unsigned)(((c * 32 + bidx) * 4 + w) * 27 + lane) * 128u : 0x7fffffffu;
          asm volatile("buffer_load_dword %0, %1, %2, 0 offen" : "+v"(pfj) : "v"(li), "s"(s2) : "memory");
        }
        if (c == 2) {
          const u32x4 s2 = mksrd(wo + 144 * kq * D, 144 * D * 2);
          const unsigned li = lane < 11 ? (unsigned)((bidx * 4 + w) * 11 + lane) * 128u : 0x7fffffffu;
          asm volatile("buffer_load_dword %0, %1, %2, 0 offen" : "+v"(pfj) : "v"(li), "s"(s2) : "memory");
        }
        if (c == 0) arrive<C_L>(aC, 2u);
        else if (c >= 3 && c <= 5) {
          if (w != kq) {
#pragma unroll
            for (int i = 0; i < 3; ++i) __builtin_amdgcn_raw_buffer_store_b128(u32x4{~0u, ~0u, ~0u, ~0u}, rsp, voff + (unsigned)(w * 9216 + (c - 3) * 3072) + i * 1024, 0, 16);
          }
        } else if (c == 6) {
          const int r = (lane >> 2) & 3;
          const int grow = 36 * (4 * kq + w) + 16 + (r >> 1) * 18 + (r & 1);
          glds16<L_EX, 0>((unsigned)((grow * D + 144 * nq + (lane >> 4) * 32 + ((lane & 3) ^ ((w & 1) << 1)) * 8) * 2), wo, lds0 + w * 1024);
          const int rho = (lane >> 1) & 15;
          const int grow2 = 36 * (4 * kq + (rho >> 2)) + 16 + ((rho >> 1) & 1) * 18 + (rho & 1);
          glds16<L_EX + 4096, 0>((unsigned)((grow2 * D + 144 * nq + 128 + (lane & 1) * 8) * 2), wo, lds0);
        } else if (c == 7) gsignal(flags + ((rb * 4 + kq) * 4 + (lane & 3)) * 32 + 8 + nq, w == 0 && lane < 4);
      }
#pragma unroll
      for (int t2 = 0; t2 < 4; ++t2) {
        ITER(2 * t2, false, kA0, kA1, vA0, vA1, kB0, kB1, vB0, vB1);
        ITER(2 * t2 + 1, true, kB0, kB1, vB0, vB1, kA0, kA1, vA0, vA1);
      }
      PIN(pfj);
      const int dlt = (c & 1) ? -CHUNK : CHUNK;
      kp0 += dlt; kp1 += dlt; vp += dlt;
    }
    ob = MFMA(vA0, BC(pb0), ob); ob = MFMA(vA1, BC(pb1), ob);
#undef ITER
#undef RESC
#undef GAP
#undef EX
    {
      float ca = 0.f, cb = 0.f;
#pragma unroll
      for (int r = 0; r < 11; ++r) { ca = __builtin_fmaf(oa[r], 0.f, ca); cb = __builtin_fmaf(ob[r], 0.f, cb); }
      const bool bad = bad0 || !(ca == 0.f) || !(cb == 0.f) || !(oa[10] > L_MIN) || !(ob[10] > L_MIN) || !(oa[10] < L_MAX) || !(ob[10] < L_MAX) || !(ma < M_MAX) || !(mb < M_MAX);
      if (__builtin_expect(__any(bad), 0)) { oa = attn_slow(kv, BC(qa0), qa1, lane); ob = attn_slow(kv, BC(qb0), qb1, lane); }
    }
    {
      const float ra = 1.0f / oa[10], rbb = 1.0f / ob[10];
      const u32x4 f0 = {cvtpk(oa[0] * ra, oa[1] * ra), cvtpk(oa[2] * ra, oa[3] * ra), cvtpk(ob[0] * rbb, ob[1] * rbb), cvtpk(ob[2] * rbb, ob[3] * rbb)};
      const u32x4 f1 = {cvtpk(oa[4] * ra, oa[5] * ra), cvtpk(oa[6] * ra, oa[7] * ra), cvtpk(ob[4] * rbb, ob[5] * rbb), cvtpk(ob[6] * rbb, ob[7] * rbb)};
      const __amdgpu_buffer_rsrc_t rso = MKRS(WS_OF + (unsigned)(mt * 32 + 2 * cg) * 1024, 2048), rsx = MKRS(WS_OX + (unsigned)(mt * 16 + cg) * 256, 256);
      const unsigned oo = (unsigned)((m >> 4) * 1024 + ((m & 15) + 16 * h) * 16);
      __builtin_amdgcn_raw_buffer_store_b128(f0, rso, oo, 0, 16);
      __builtin_amdgcn_raw_buffer_store_b128(f1, rso, oo + 512, 0, 16);
      const u32x2 ex = {cvtpk(oa[8] * ra, oa[9] * ra), cvtpk(ob[8] * rbb, ob[9] * rbb)};
      if (h == 0) __builtin_amdgcn_raw_buffer_store_b64(ex, rsx, m * 8, 0, 16);
      waitv<0>();
      gsignal(flags + (mt * 4 + kq) * 32 + 4 + nq, lane == 0);
    }
    __builtin_amdgcn_s_setprio(0);
    {
      const int fm = rb * 4 + kq;
      const int p0 = w == 3 ? 6 : 2 * w, np = w == 3 ? 3 : 2;
      const int g4 = lane >> 4;
      const size_t rowoff = (size_t)(32 * fm + 16 * (g4 & 1) + (lane & 15)) * D + 144 * nq;
      u32x4 xv[3], bv[3];
#pragma unroll
      for (int i = 0; i < 3; ++i) { const int pi = i < np ? p0 + i : p0; xv[i] = *(const u32x4*)(x + rowoff + 16 * pi + 8 * h); bv[i] = *(const u32x4*)(b2 + 144 * nq + 16 * pi + 8 * h); }
      lds_wait(lds0 + L_SYNC + SY_F);
      u32x4 t[3][3];
      auto pload = [&]() {
#pragma unroll
        for (int q = 0; q < 3; ++q) {
          const int src = q < kq ? q : q + 1;
#pragma unroll
          for (int i = 0; i < 3; ++i) { const int pi = i < np ? p0 + i : p0; t[q][i] = __builtin_amdgcn_raw_buffer_load_b128(rsp, voff, (src * 9 + pi) * 1024, 16); }
        }
      };
      pload();
      SBAR();
      float base[3][8];
#pragma unroll
      for (int i = 0; i < 3; ++i) {
        const int pi = i < np ? p0 + i : p0;
        const f32x4 o0 = *(const LAS f32x4*)((lds_ptr)smem + L_KV + pi * 2048 + voff), o1 = *(const LAS f32x4*)((lds_ptr)smem + L_KV + pi * 2048 + 1024 + voff);
#pragma unroll
        for (int e = 0; e < 4; ++e) {
          auto r = __builtin_amdgcn_permlane16_swap(__float_as_uint(o0[e]), __float_as_uint(o1[e]), false, false);
          const unsigned xa = xv[i][e >> 1], ba = bv[i][e >> 1], xb = xv[i][2 + (e >> 1)], bb = bv[i][2 + (e >> 1)];
          base[i][e] = __uint_as_float(r[0]) + ((e & 1) ? bf_hi(xa) + bf_hi(ba) : bf_lo(xa) + bf_lo(ba));
          base[i][4 + e] = __uint_as_float(r[1]) + ((e & 1) ? bf_hi(xb) + bf_hi(bb) : bf_lo(xb) + bf_lo(bb));
        }
      }
      SBAR();
#pragma unroll 1
      for (int it = 0; it < (1 << 14); ++it) {
        unsigned mx = 0u;
#pragma unroll
        for (int q = 0; q < 3; ++q)
#pragma unroll
          for (int i = 0; i < 3; ++i)
#pragma unroll
            for (int e = 0; e < 4; ++e) mx = mx > t[q][i][e] ? mx : t[q][i][e];
        if (!__any(mx == ~0u)) break;
        asm volatile("" ::: "memory");
        pload();
      }
#pragma unroll
      for (int i = 0; i < 3; ++i) {
        u32x4 o;
#pragma unroll
        for (int e = 0; e < 4; ++e)
          o[e] = cvtpk(((base[i][2 * e] + bf_lo(t[0][i][e])) + bf_lo(t[1][i][e])) + bf_lo(t[2][i][e]), ((base[i][2 * e + 1] + bf_hi(t[0][i][e])) + bf_hi(t[1][i][e])) + bf_hi(t[2][i][e]));
        if (i < np) *(u32x4*)(out + rowoff + 16 * (p0 + i) + 8 * h) = o;
      }
    }
  }
}

extern "C" void kernel_launch(void* const* d_in, const int* in_sizes, int n_in, void* d_out, int out_size, void* d_ws, size_t ws_size, hipStream_t stream) {
  const u16* x = (const u16*)d_in[0];
  const u16* wq = (const u16*)d_in[1];
  const u16* wk = (const u16*)d_in[2];
  const u16* wv = (const u16*)d_in[3];
  const u16* wo = (const u16*)d_in[4];
  const u16* w1 = (const u16*)d_in[6];
  const u16* b1 = (const u16*)d_in[7];
  const u16* w2 = (const u16*)d_in[8];
  const u16* b2 = (const u16*)d_in[9];
  char* ws = (char*)d_ws;
  (void)hipFuncSetAttribute((const void*)k_main, hipFuncAttributeMaxDynamicSharedMemorySize, L_TOTAL);
  k_ln_kv<<<256, 384, 0, stream>>>(x, wk, wv, wq, w1, b1, ws);
  k_main<<<256, 512, L_TOTAL, stream>>>(x, wo, w1, b1, w2, b2, ws, (u16*)d_out);
}
```

```cpp
#include <hip/hip_runtime.h>
#include <hip/hip_bf16.h>
#include <stdint.h>
#include <type_traits>
#define T1S 0
#define GS0 8
#define KSL 8
#define LNW 6u
#define BS0 20
#define XS0 16
#define XSL 6
#define STS 12
#define PFS 0

typedef unsigned short u16;
using bf16x8 = __attribute__((ext_vector_type(8))) short;
using s16x4 = __attribute__((ext_vector_type(4))) short;
using f32x16 = __attribute__((ext_vector_type(16))) float;
using f32x4 = __attribute__((ext_vector_type(4))) float;
using u32x4 = __attribute__((ext_vector_type(4))) unsigned;
using u32x2 = __attribute__((ext_vector_type(2))) unsigned;
typedef float f32x2_t __attribute__((ext_vector_type(2)));
typedef __bf16 bf16x2_t __attribute__((ext_vector_type(2)));
#define LAS __attribute__((address_space(3)))
#define GAS __attribute__((address_space(1)))
typedef LAS char* lds_ptr;
typedef GAS unsigned gu32;
typedef short v4i16_t __attribute__((ext_vector_type(4)));

#define DI __device__ __forceinline__
#define SBAR() __builtin_amdgcn_sched_barrier(0)
#define PIN(x) asm volatile("" : "+v"(x))
#define PINS(x) asm volatile("" : "+s"(x))
#define MFMA(a, b, c) __builtin_amdgcn_mfma_f32_32x32x16_bf16((a), (b), (c), 0, 0, 0)
#define BC(x) __builtin_bit_cast(bf16x8, x)
#define RLX_AGENT __ATOMIC_RELAXED, __HIP_MEMORY_SCOPE_AGENT

constexpr int D = 576, DH = 18, F = 4608;
constexpr float C2 = 0.23570226039551584f * 1.4426950408889634f;

constexpr unsigned WS_FLAGS = 0;
constexpr unsigned FL_E = 256 * 128;
constexpr int SL_XN = 0;
constexpr int SL_KV = 64;
constexpr int SL_ACK = 72;
constexpr unsigned FL_BEAT = 65536;
constexpr unsigned eword(int slot0, int i) { return (FL_E + (unsigned)(slot0 + (i >> 4)) * 256u) / 4u + (unsigned)(i & 15); }
constexpr unsigned WS_XNF = 131072;
constexpr unsigned WS_KV = WS_XNF + 64 * 36 * 1024;
constexpr unsigned WS_OF = WS_KV + 196608;
constexpr unsigned WS_OX = WS_OF + 64 * 32 * 1024;
constexpr unsigned WS_HF = WS_OX + 64 * 16 * 256;
constexpr unsigned WS_PART = WS_HF + 64 * 288 * 1024;

constexpr int REC = 2560, CHUNK = 8 * REC;
constexpr int GSLOT = 36864;
constexpr int L_EX = 0, L_BIAS = 5120, L_SYNC = L_BIAS + 1152, L_G = 6400, L_KV = L_G + 3 * GSLOT, L_CNT = L_KV + 2 * CHUNK  , L_TOTAL = L_CNT + 768;
constexpr int L_Q = L_KV + CHUNK, QROW = 88;
constexpr int L_Q1 = L_Q + 128 * QROW;
constexpr int SY_LN = 0, SY_A = 4, SY_Q = 8  , SY_F = 24, SY_GO = 28, SY_KVW = 32, SY_KV = 36, SY_ST = 40, SY_Z = 44, SY_ST2 = 48, SY_XN = 52;
constexpr int L_LN = L_G + 2 * GSLOT;

DI unsigned cvtpk(float lo, float hi) { f32x2_t v = {lo, hi}; bf16x2_t b = __builtin_convertvector(v, bf16x2_t); return __builtin_bit_cast(unsigned, b); }
DI float bf_lo(unsigned u) { return __uint_as_float(u << 16); }
DI float bf_hi(unsigned u) { return __uint_as_float(u & 0xffff0000u); }
DI float bfr(float x) { return bf_lo(cvtpk(x, x) & 0xffffu); }
DI s16x4 trread(lds_ptr p) { return __builtin_bit_cast(s16x4, __builtin_amdgcn_ds_read_tr16_b64_v4i16((LAS v4i16_t*)p)); }
DI bf16x8 cat8(s16x4 a, s16x4 b) { return __builtin_shufflevector(a, b, 0, 1, 2, 3, 4, 5, 6, 7); }
template <int LI, int GI>
DI void glds16(unsigned voff, const void* sbase, unsigned ldsw) {
  asm volatile("s_add_u32 m0, %2, %3\n\ts_nop 0\n\tglobal_load_lds_dwordx4 %0, %1 offset:%4 sc1"
               :: "v"(voff), "s"(sbase), "s"(ldsw), "i"(LI - GI), "i"(GI) : "memory", "scc");
}
DI u32x4 mksrd(const void* p, unsigned bytes) { const unsigned long long a = (unsigned long long)p; return u32x4{(unsigned)a, (unsigned)(a >> 32) & 0xffffu, bytes, 0x00020000u}; }
template <int LI>
DI void bdma16(unsigned voff, u32x4 srd, unsigned soff, unsigned ldsw) {
  asm volatile("s_add_u32 m0, %3, %4\n\ts_nop 0\n\tbuffer_load_dwordx4 %0, %1, %2 offen lds"
               :: "v"(voff), "s"(srd), "s"(soff), "s"(ldsw), "i"(LI) : "memory", "scc");
}
template <int OFF>
DI void bload16_sc1(u32x4& dst, unsigned voff, u32x4 srd, unsigned soff) {
  asm volatile("buffer_load_dwordx4 %0, %1, %2, %3 offen offset:%4 sc1" : "=v"(dst) : "v"(voff), "s"(srd), "s"(soff), "i"(OFF) : "memory");
}
template <int OFF>
DI void gload16(bf16x8& dst, unsigned voff, const void* sbase) { asm volatile("global_load_dwordx4 %0, %1, %2 offset:%3 sc1" : "=v"(dst) : "v"(voff), "s"(sbase), "i"(OFF) : "memory"); }
template <int N> DI void waitv() { asm volatile("s_waitcnt vmcnt(%c0)" :: "i"(N) : "memory"); }
template <int OFF> DI void arrive(unsigned addr, unsigned one) { asm volatile("ds_add_u32 %0, %1 offset:%2" :: "v"(addr), "v"(one), "i"(OFF) : "memory"); }
template <int OFF> DI void check(unsigned v, unsigned addr, unsigned target) {
  asm volatile(
      "v_cmp_gt_u32 vcc, %2, %0\n\t"
      "s_cbranch_vccz 2f\n\t"
      "1:\n\t"
      "ds_read_b32 %0, %1 offset:%3\n\t"
      "s_waitcnt lgkmcnt(0)\n\t"
      "v_cmp_gt_u32 vcc, %2, %0\n\t"
      "s_cbranch_vccnz 1b\n\t"
      "2:"
      : "+v"(v) : "v"(addr), "s"(target), "i"(OFF) : "memory", "vcc");
}
constexpr int C_R = 0, C_L = 256, C_L0 = 512;
template <int LI> DI void set_m0(unsigned ldsw) { asm volatile("s_add_u32 m0, %0, %1" :: "s"(ldsw), "i"(LI) : "memory", "scc"); }
template <int OFF> DI void bdma_m0(unsigned voff, u32x4 srd, unsigned soff) {
  asm volatile("buffer_load_dwordx4 %0, %1, %2 offen offset:%3 lds" :: "v"(voff), "s"(srd), "s"(soff), "i"(OFF) : "memory");
}

#define TS(kid, i) do {} while (0)
template <int VM>
DI void swbar(unsigned addr, unsigned target) {
  unsigned v, s;
  asm volatile(
      "s_waitcnt vmcnt(%c4) lgkmcnt(0)\n\t"
      "s_mov_b64 exec, 1\n\t"
      "v_mov_b32 %0, 1\n\t"
      "ds_add_rtn_u32 %0, %2, %0\n\t"
      "s_waitcnt lgkmcnt(0)\n\t"
      "v_add_u32 %0, 1, %0\n\t"
      "1:\n\t"
      "s_nop 0\n\t"
      "v_readfirstlane_b32 %1, %0\n\t"
      "s_nop 1\n\t"
      "s_cmp_ge_u32 %1, %3\n\t"
      "s_cbranch_scc1 2f\n\t"
      "ds_read_b32 %0, %2\n\t"
      "s_waitcnt lgkmcnt(0)\n\t"
      "s_branch 1b\n\t"
      "2:\n\t"
      "s_mov_b64 exec, -1"
      : "=&v"(v), "=&s"(s) : "v"(addr), "s"(target), "i"(VM) : "memory", "scc");
}
DI void lds_wait(unsigned addr) {
  unsigned v, s;
  asm volatile(
      "1:\n\t"
      "ds_read_b32 %0, %2\n\t"
      "s_waitcnt lgkmcnt(0)\n\t"
      "v_readfirstlane_b32 %1, %0\n\t"
      "s_nop 1\n\t"
      "s_cmp_lg_u32 %1, 0\n\t"
      "s_cbranch_scc1 2f\n\t"
      "s_sleep 2\n\t"
      "s_branch 1b\n\t"
      "2:"
      : "=&v"(v), "=&s"(s) : "v"(addr) : "memory", "scc");
}
DI unsigned lds_inc(unsigned addr) {
  unsigned v;
  asm volatile("s_waitcnt vmcnt(0) lgkmcnt(0)\n\ts_mov_b64 exec, 1\n\tv_mov_b32 %0, 1\n\tds_add_rtn_u32 %0, %1, %0\n\ts_waitcnt lgkmcnt(0)\n\ts_mov_b64 exec, -1" : "=&v"(v) : "v"(addr) : "memory");
  return __builtin_amdgcn_readfirstlane(v);
}
DI unsigned lds_inc_nw(unsigned addr) {
  unsigned v;
  asm volatile("s_waitcnt lgkmcnt(0)\n\ts_mov_b64 exec, 1\n\tv_mov_b32 %0, 1\n\tds_add_rtn_u32 %0, %1, %0\n\ts_waitcnt lgkmcnt(0)\n\ts_mov_b64 exec, -1" : "=&v"(v) : "v"(addr) : "memory");
  return __builtin_amdgcn_readfirstlane(v);
}
DI void lds_wait_ge(unsigned addr, unsigned target) {
  unsigned v, s;
  asm volatile(
      "1:\n\t"
      "ds_read_b32 %0, %2\n\t"
      "s_waitcnt lgkmcnt(0)\n\t"
      "v_readfirstlane_b32 %1, %0\n\t"
      "s_nop 1\n\t"
      "s_cmp_ge_u32 %1, %3\n\t"
      "s_cbranch_scc1 2f\n\t"
      "s_sleep 2\n\t"
      "s_branch 1b\n\t"
      "2:"
      : "=&v"(v), "=&s"(s) : "v"(addr), "s"(target) : "memory", "scc");
}
DI void lds_set(unsigned addr) {
  unsigned v;
  asm volatile("s_waitcnt lgkmcnt(0)\n\tv_mov_b32 %0, 1\n\tds_write_b32 %1, %0" : "=&v"(v) : "v"(addr) : "memory");
}
template <int SL>
DI unsigned gpoll(gu32* p, unsigned need) {
  unsigned got = 0u;
#pragma unroll 1
  for (unsigned i = 0; i < (1u << 18); ++i) {
    got = (unsigned)__ballot(__hip_atomic_load(p, RLX_AGENT) != 0u);
    if ((got & need) == need) break;
    __builtin_amdgcn_s_sleep(SL);
  }
  __builtin_amdgcn_fence(__ATOMIC_ACQUIRE, "wavefront");
  asm volatile("" ::: "memory");
  return got;
}
DI void gsignal(gu32* p, bool on) { if (on) __hip_atomic_store(p, 1u, RLX_AGENT); }

#define MKRS(off, bytes) __builtin_amdgcn_make_buffer_rsrc((void*)(ws + (off)), 0, (int)(bytes), 0x00020000)
#define M16(a, b, c) __builtin_amdgcn_mfma_f32_16x16x32_bf16((a), (b), (c), 0, 0, 0)
DI float xg_sum(float v) {
  auto r = __builtin_amdgcn_permlane16_swap(__float_as_uint(v), __float_as_uint(v), false, false);
  v = __uint_as_float(r[0]) + __uint_as_float(r[1]);
  auto q = __builtin_amdgcn_permlane32_swap(__float_as_uint(v), __float_as_uint(v), false, false);
  return __uint_as_float(q[0]) + __uint_as_float(q[1]);
}
DI void ln_kv(const u16* __restrict__ x, const u16* __restrict__ wk, const u16* __restrict__ wv, char* __restrict__ ws, lds_ptr smem, int mt, int w, int lane) {
  const int m = lane & 15, g = lane >> 4;
  const unsigned lds0 = (unsigned)(uintptr_t)smem;
  const u16* xrow = x + (size_t)(16 * mt + m) * D;
  u32x4 xr[3];
#pragma unroll
  for (int i = 0; i < 3; ++i) xr[i] = *(const u32x4*)(xrow + 32 * (3 * w + i) + 8 * g);
  unsigned c0u = *(const unsigned*)xrow;
  typedef unsigned u32x4u __attribute__((ext_vector_type(4), aligned(4)));
  u32x4 ra[3], rb[3]; unsigned rc[3];
#pragma unroll
  for (int i = 0; i < 3; ++i) {
    const int r = lane + 64 * i;
    const char* src = (r < 96 ? (const char*)wk + r * 36 : (const char*)wv + (r - 96) * 36) + w * 3456;
    ra[i] = *(const u32x4u*)src; rb[i] = *(const u32x4u*)(src + 16); rc[i] = *(const unsigned*)(src + 32);
  }
  if (w == 1) __hip_atomic_store((gu32*)(ws + WS_FLAGS) + mt * 64 + lane, 0u, RLX_AGENT);
  SBAR();
  asm volatile("s_barrier" ::: "memory");
  SBAR();
  asm volatile("" : "+v"(xr[0]), "+v"(xr[1]), "+v"(xr[2]), "+v"(c0u));
  const float c0 = __uint_as_float(c0u << 16);
  const lds_ptr im = smem + L_LN + w * 9216;
  LAS float* stats = (LAS float*)(smem + L_EX);
  float v[24], s = 0.f, q = 0.f;
#pragma unroll
  for (int i = 0; i < 3; ++i)
#pragma unroll
    for (int j = 0; j < 4; ++j) { v[8 * i + 2 * j] = bf_lo(xr[i][j]); v[8 * i + 2 * j + 1] = bf_hi(xr[i][j]); }
#pragma unroll
  for (int i = 0; i < 24; ++i) { const float d = v[i] - c0; s += d; q = __builtin_fmaf(d, d, q); }
  s = xg_sum(s); q = xg_sum(q);
  if (g == 0) *(LAS f32x2_t*)&stats[m * 12 + 2 * w] = f32x2_t{s, q};
  swbar<63>(lds0 + L_SYNC + SY_LN, 6u);
  s = 0.f; q = 0.f;
#pragma unroll
  for (int i = 0; i < 3; ++i) { const f32x4 t = *(const LAS f32x4*)&stats[m * 12 + 4 * i]; s += t[0] + t[2]; q += t[1] + t[3]; }
  const float md = s * (1.f / 576.f);
  const float var = __builtin_fmaxf(__builtin_fmaf(-md, md, q * (1.f / 576.f)), 0.f);
  const float mb = bfr(c0 + md), sd = bfr(__builtin_amdgcn_sqrtf(bfr(bfr(var) + 1e-5f)));
  const float rinv = __builtin_amdgcn_rcpf(sd);
  u32x4 o[3];
  const __amdgpu_buffer_rsrc_t rsx = MKRS(WS_XNF + (unsigned)(mt * 18 + 3 * w) * 1024, 3072);
#pragma unroll
  for (int i = 0; i < 3; ++i) {
#pragma unroll
    for (int j = 0; j < 4; ++j) {
      const unsigned dp = cvtpk(v[8 * i + 2 * j] - mb, v[8 * i + 2 * j + 1] - mb);
      o[i][j] = cvtpk(bf_lo(dp) * rinv, bf_hi(dp) * rinv);
    }
    __builtin_amdgcn_raw_buffer_store_b128(o[i], rsx, i * 1024 + lane * 16, 0, 16);
  }
  SBAR();
#pragma unroll
  for (int i = 0; i < 3; ++i) {
    const lds_ptr dst = im + (lane + 64 * i) * 48;
    *(LAS u32x4*)dst = ra[i]; *(LAS u32x4*)(dst + 16) = rb[i]; *(LAS unsigned*)(dst + 32) = rc[i];
  }
  const lds_ptr rd = im + (8 * g + (m >> 2)) * 48 + (m & 3) * 8, rd1 = im + (8 * g + (m >> 2)) * 48 + 32;
  f32x4 acc[4] = {};
#pragma unroll
  for (int i = 0; i < 3; ++i) {
    acc[0] = M16(cat8(trread(rd + i * 1536), trread(rd + i * 1536 + 192)), BC(o[i]), acc[0]);
    acc[1] = M16(cat8(trread(rd1 + i * 1536), trread(rd1 + i * 1536 + 192)), BC(o[i]), acc[1]);
    acc[2] = M16(BC(o[i]), cat8(trread(rd + 4608 + i * 1536), trread(rd + 4608 + i * 1536 + 192)), acc[2]);
    acc[3] = M16(BC(o[i]), cat8(trread(rd1 + 4608 + i * 1536), trread(rd1 + 4608 + i * 1536 + 192)), acc[3]);
  }
#pragma unroll
  for (int i = 0; i < 4; ++i) *(LAS f32x4*)(im + i * 1024 + lane * 16) = acc[i];
  swbar<63>(lds0 + L_SYNC + SY_LN, 12u);
  gu32* flags = (gu32*)(ws + WS_FLAGS);
  if (w < 4) {
    f32x4 a = {};
#pragma unroll
    for (int ww = 0; ww < 6; ++ww) a += *(const LAS f32x4*)(smem + L_LN + ww * 9216 + w * 1024 + lane * 16);
    u32x2 p = {cvtpk(a[0], a[1]), cvtpk(a[2], a[3])};
    const __amdgpu_buffer_rsrc_t rsk = MKRS(WS_KV + (unsigned)(mt >> 1) * REC, 2 * REC);
    const int key = 16 * (mt & 1) + m, dc = (w - 2) * 16 + m;
    if (w == 0) __builtin_amdgcn_raw_buffer_store_b64(p, rsk, (key + 32 * (g & 1)) * 16 + (g >> 1) * 8, 0, 16);
    else if (w == 1) { if (g == 0) __builtin_amdgcn_raw_buffer_store_b32(p[0], rsk, 1024 + key * 4, 0, 16); }
    else {
      if (dc == DH) p = u32x2{0x3f803f80u, 0x3f803f80u};
      if (dc <= DH) __builtin_amdgcn_raw_buffer_store_b64(p, rsk, REC + 1152 + ((mt & 1) * 2 + (g & 1)) * 304 + dc * 16 + (g >> 1) * 8, 0, 16);
    }
  }
  lds_wait(lds0 + L_SYNC + SY_GO);
  if (w < 4) waitv<1>(); else waitv<0>();
  gsignal(flags + eword(SL_XN, (mt >> 1) * 16 + (mt & 1) * 6 + w), lane == 0);
  if (w < 4) {
    asm volatile("s_waitcnt vmcnt(1)" ::: "memory");
    if (lds_inc_nw(lds0 + L_SYNC + SY_KVW) == 3u) gsignal(flags + eword(SL_KV, mt), lane == 0);
  }
}

constexpr int M_OFF = 90;
constexpr float L_TRIG = 9.5367431640625e-07f;
constexpr float L_MAX = 1.3292280e36f;
constexpr float L_MIN = 1e-29f;
constexpr float M_MAX = 4194304.f;

DI void set_negm(u32x2& q1, float m, int h) {
  const float nm = -m;
  const unsigned hi = __float_as_uint(nm) & 0xffff0000u;
  const float r1 = nm - __uint_as_float(hi);
  const unsigned mid = __float_as_uint(r1) & 0xffff0000u;
  const float r2 = r1 - __uint_as_float(mid);
  q1[1] = h ? (__float_as_uint(r2) >> 16) : ((hi >> 16) | mid);
}
DI float half_max(float v) {
  auto rr = __builtin_amdgcn_permlane32_swap(__float_as_uint(v), __float_as_uint(v), false, false);
  return __builtin_fmaxf(__uint_as_float(rr[0]), __uint_as_float(rr[1]));
}
DI float tile_max(const f32x16& s) {
  float a = __builtin_fmaxf(s[0], s[1]);
#pragma unroll
  for (int r = 2; r < 16; ++r) a = __builtin_fmaxf(a, s[r]);
  return half_max(a);
}
#define MFMA8(a, b) __builtin_amdgcn_mfma_f32_32x32x8bf16_1k(__builtin_bit_cast(s16x4, a), __builtin_bit_cast(s16x4, b), f32x16{}, 0, 0, 0)
DI f32x16 attn_slow(const char* kv, bf16x8 q0, u32x2 q1, int lane) {
  const int m = lane & 31, h = lane >> 5, vr = m < DH ? m : DH;
  q1[1] = 0u;
  __builtin_amdgcn_fence(__ATOMIC_ACQUIRE, "agent");
  float mx = -__builtin_inff();
  f32x16 o = f32x16{};
#pragma unroll 1
  for (int t = 0; t < 64; ++t) {
    const char* rk = kv + (size_t)t * REC; const char* rv = rk + REC + 1152 + h * 304 + vr * 16;
    const bf16x8 k0 = *(const bf16x8*)(rk + lane * 16);
    const u32x2 k1 = {*(const unsigned*)(rk + 1024 + m * 4), 0u};
    const bf16x8 v0 = *(const bf16x8*)rv, v1 = *(const bf16x8*)(rv + 608);
    f32x16 s = MFMA8(k1, q1);
    s = MFMA(k0, q0, s);
    const float mn = __builtin_fmaxf(mx, tile_max(s));
    const float al = __builtin_amdgcn_exp2f(mx - mn);
    mx = mn;
    u32x4 p0, p1;
#pragma unroll
    for (int j = 0; j < 4; ++j) {
      p0[j] = cvtpk(__builtin_amdgcn_exp2f(s[2 * j] - mn), __builtin_amdgcn_exp2f(s[2 * j + 1] - mn));
      p1[j] = cvtpk(__builtin_amdgcn_exp2f(s[8 + 2 * j] - mn), __builtin_amdgcn_exp2f(s[8 + 2 * j + 1] - mn));
    }
#pragma unroll
    for (int r = 0; r < 16; ++r) o[r] *= al;
    o = MFMA(v0, BC(p0), o);
    o = MFMA(v1, BC(p1), o);
  }
  return o;
}
DI unsigned relu2pk(float a, float b) {
  unsigned r; float t;
  asm volatile("v_mul_f32_e64 %0, %2, |%2|\n\tv_mul_f32_e64 %1, %3, |%3|\n\tv_cvt_pk_bf16_f32 %0, %0, %1\n\tv_pk_max_i16 %0, %0, 0" : "=&v"(r), "=&v"(t) : "v"(a), "v"(b));
  return r;
}
DI void mfma_dc(f32x4& d, bf16x8 a, bf16x8 b, const f32x4& c) { asm volatile("v_mfma_f32_16x16x32_bf16 %0, %1, %2, %3" : "=&v"(d) : "v"(a), "v"(b), "v"(c)); }
DI void mfma_acc(f32x4& c, bf16x8 a, bf16x8 b) { asm volatile("v_mfma_f32_16x16x32_bf16 %0, %1, %2, %0" : "+v"(c) : "v"(a), "v"(b)); }
DI void mfma_z(f32x4& d, bf16x8 a, bf16x8 b) { asm volatile("v_mfma_f32_16x16x32_bf16 %0, %1, %2, 0" : "=&v"(d) : "v"(a), "v"(b)); }
template <int OFF>
DI void bload8_sc1(u32x2& dst, unsigned voff, u32x4 srd) { asm volatile("buffer_load_dwordx2 %0, %1, %2, 0 offen offset:%3 sc1" : "=v"(dst) : "v"(voff), "s"(srd), "i"(OFF) : "memory"); }

template <int N, class Fn> DI void static_for(Fn&& f) {
  if constexpr (N > 0) { static_for<N - 1>(f); f(std::integral_constant<int, N - 1>{}); }
}

extern "C" __global__ __launch_bounds__(512, 2) void k_main(const u16* __restrict__ x, const u16* __restrict__ wq, const u16* __restrict__ wk, const u16* __restrict__ wv, const u16* __restrict__ wo, const u16* __restrict__ w1,
                                                 const u16* __restrict__ b1, const u16* __restrict__ w2, const u16* __restrict__ b2, char* __restrict__ ws, u16* __restrict__ out) {
  extern __shared__ __attribute__((aligned(16))) char smem[];
  unsigned long long pc0;
  asm volatile("s_getpc_b64 %0" : "=s"(pc0));
  SBAR();
  pc0 &= ~255ull;
  const int xcd = blockIdx.x & 7, bidx = blockIdx.x >> 3;
  const int kq = xcd & 3, nq = bidx & 3, cg = kq * 4 + nq, rb = (xcd >> 2) * 8 + (bidx >> 2);
  const int tid = threadIdx.x, lane = tid & 63, m = lane & 31, h = lane >> 5;
  const int w8 = __builtin_amdgcn_readfirstlane(tid >> 6), w = w8 & 3;
  const int mt = rb * 4 + w;
  const unsigned lds0 = (unsigned)(uintptr_t)(lds_ptr)smem;
  const unsigned voff = lane * 16;
  const int n0q = (36 * cg) & ~7;
  gu32* flags = (gu32*)(ws + WS_FLAGS);

  const bool lnblk = (nq & 1) == 0, leader = blockIdx.x == 8;
  if (w8 == 0) TS(0, 0);
  if (w8 < 6) {
    if (lnblk) ln_kv(x, wk, wv, ws, (lds_ptr)smem, rb * 8 + kq * 2 + (nq >> 1), w8, lane);
    else {
      if (leader) {
        __builtin_amdgcn_raw_buffer_store_b128(u32x4{0u, 0u, 0u, 0u}, MKRS(WS_FLAGS + FL_E, 96 * 256), (unsigned)((w8 * 16 + (lane >> 2)) * 256 + (lane & 3) * 16), 0, 16);
        if (w8 < 2) __builtin_amdgcn_raw_buffer_store_b128(u32x4{0u, 0u, 0u, 0u}, MKRS(WS_KV + 64 * REC, 1152), voff + (unsigned)w8 * 1024u, 0, 16);
      }
      asm volatile("s_barrier" ::: "memory");
      if (leader) { (void)lds_inc(lds0 + L_SYNC + SY_Z); lds_wait(lds0 + L_SYNC + SY_GO); __builtin_amdgcn_s_sleep(24); }
      __builtin_amdgcn_s_sleep(PFS);
      const u32x4 s1 = mksrd(w1, D * F * 2);
      const int id = ((bidx >> 2) * 2 + (nq >> 1)) * 384 + w8 * 64 + lane;
      const int li = (xcd >> 2) * 5184 + id, row = li / 18;
      unsigned j0, j1;
      asm volatile("global_load_dword %1, %4, %5\n\tbuffer_load_dword %0, %2, %3, 0 offen\n\ts_waitcnt vmcnt(0)"
                   : "=&v"(j0), "=&v"(j1) : "v"(id < 5184 ? (unsigned)(row * F + 1152 * kq + (li - row * 18) * 64) * 2u : 0x7fffffffu), "s"(s1),
                     "v"((unsigned)((w8 * 64 + lane) % 200) * 128u), "s"(pc0) : "memory");
    }
  } else {
    gu32* beat = flags + (FL_BEAT + (blockIdx.x & 31) * 256) / 4;
    const unsigned b0 = __hip_atomic_load(beat, RLX_AGENT);
    const int t2 = tid & 127;
    if (t2 < 16) *(unsigned*)(smem + L_SYNC + t2 * 4) = 0u;
    *(unsigned*)(smem + L_CNT + t2 * 4) = 0u;
    if (t2 < 64) *(unsigned*)(smem + L_CNT + 512 + t2 * 4) = 0u;
    asm volatile("s_waitcnt lgkmcnt(0)\n\ts_barrier" ::: "memory");
    if (w8 == 7) {
      if (leader) {
        lds_wait_ge(lds0 + L_SYNC + SY_Z, 6u);
        lds_set(lds0 + L_SYNC + SY_GO);
        gsignal(flags + eword(SL_ACK, blockIdx.x), lane == 0);
        unsigned bt = (unsigned)__builtin_amdgcn_s_memrealtime();
#pragma unroll 1
        for (unsigned i = 0; i < (1u << 18); ++i) {
          if (lane < 32) __hip_atomic_store(flags + (FL_BEAT + lane * 256) / 4, ++bt, RLX_AGENT);
          __builtin_amdgcn_s_sleep(8);
          unsigned ok = 1u;
#pragma unroll
          for (int k = 0; k < 4; ++k) ok &= __hip_atomic_load(flags + eword(SL_ACK, k * 64 + lane), RLX_AGENT);
          if (__all(ok != 0u)) break;
        }
      } else {
        __builtin_amdgcn_s_sleep(BS0);
#pragma unroll 1
        for (unsigned i = 0; i < (1u << 18); ++i) {
          if (__builtin_amdgcn_readfirstlane(__hip_atomic_load(beat, RLX_AGENT)) != __builtin_amdgcn_readfirstlane(b0)) break;
          __builtin_amdgcn_s_sleep(8);
        }
        gsignal(flags + eword(SL_ACK, blockIdx.x), lane == 0);
      }
      lds_set(lds0 + L_SYNC + SY_GO);
      TS(3, 0);
    }
  }
  if (w8 >= 4) {
    const int prow = lane >> 2, pch = (lane & 3) ^ ((lane >> 5) << 1);
    if (w8 == 6) {
      if (leader) lds_wait(lds0 + L_SYNC + SY_GO);
      __builtin_amdgcn_s_sleep(STS);
      unsigned bv[5];
#pragma unroll
      for (int i = 0; i < 5; ++i) bv[i] = b1[288 * cg + (lane + 64 * i < 288 ? lane + 64 * i : 0)];
      const u32x4 srdQ = mksrd(wq, D * D * 2);
      const unsigned vq0 = (unsigned)((prow * D + n0q + pch * 8) * 2), vq1 = (unsigned)((lane * D + n0q + 32) * 2);
#pragma unroll 1
      for (int q = 0; q < 9; ++q) {
        static_for<4>([&](auto i) { bdma16<L_G + i * 1024>(vq0, srdQ, (unsigned)((4 * q + i) * 16 * D * 2), lds0 + q * 4096); });
        bdma16<L_Q1>(vq1, srdQ, (unsigned)(q * 64 * D * 2), lds0 + q * 1024);
      }
      float* biasT = (float*)(smem + L_BIAS);
#pragma unroll
      for (int i = 0; i < 5; ++i) if (lane + 64 * i < 288) biasT[lane + 64 * i] = __uint_as_float(bv[i] << 16);
      (void)lds_inc(lds0 + L_SYNC + SY_ST);
      TS(3, 3);
    }
    if (lnblk) lds_wait_ge(lds0 + L_SYNC + SY_KVW, 4u); else lds_wait(lds0 + L_SYNC + SY_GO);
    const u32x4 srd1 = mksrd(w1, D * F * 2);
    const unsigned v1 = (unsigned)((prow * F + pch * 8 + 288 * cg) * 2);
    static_for<9>([&](auto p) { bdma16<L_G + GSLOT + p * 1024>(v1, srd1, (unsigned)((9 * w + p) * 16 * F * 2), lds0 + 9 * w * 1024); });
    (void)lds_inc(lds0 + L_SYNC + SY_ST2);
    if (w8 == 6) TS(3, 1);
    if (lnblk) __builtin_amdgcn_s_sleep(T1S);
    static_for<9>([&](auto p) { bdma16<L_G + 2 * GSLOT + p * 1024>(v1 + 64u, srd1, (unsigned)((9 * w + p) * 16 * F * 2), lds0 + 9 * w * 1024); });
    waitv<0>();
    arrive<C_L>(lds0 + L_CNT + lane * 4, 2u);
  }
  if (leader && w8 == 3) {
    __builtin_amdgcn_s_sleep(GS0);
#pragma unroll 1
    for (unsigned i = 0; i < (1u << 18); ++i) {
      const unsigned f0 = __hip_atomic_load(flags + eword(SL_KV, lane), RLX_AGENT), f1 = __hip_atomic_load(flags + eword(SL_KV, 64 + lane), RLX_AGENT);
      if (__all((f0 & f1) != 0u)) break;
      __builtin_amdgcn_s_sleep(4);
    }
    gsignal(flags + eword(SL_XN, lane * 16 + 12), true);
    lds_set(lds0 + L_SYNC + SY_KV);
  }
  if (leader && w8 < 3) lds_wait(lds0 + L_SYNC + SY_KV);
  if (w8 == 4) {
    lds_wait(lds0 + L_SYNC + SY_XN);
    if (__builtin_amdgcn_readfirstlane(*(volatile LAS unsigned*)((lds_ptr)smem + L_SYNC + SY_XN)) < 2u) (void)gpoll<KSL>(flags + eword(SL_XN, mt * 16 + (lane & 15)), 0x1000u);
    lds_set(lds0 + L_SYNC + SY_KV);
    TS(3, 2);
  }
  if (w8 == 0) TS(0, 1);

  if (w8 < 4) {
    const int prow = lane >> 2, pch = (lane & 3) ^ ((lane >> 5) << 1);
    const int g4 = lane >> 4, i16 = lane & 15;
    const unsigned ldsw = lds0 + L_G + 9 * w * 1024;
    bf16x8 xf[36];
    const char* xb = ws + WS_XNF + (size_t)mt * 36 * 1024;
    auto xload = [&](auto uc) { constexpr int u = uc, o = ((u & 1) * 18 + (u >> 1)) * 1024; gload16<(o & 4095)>(xf[u], voff, xb + (o & ~4095)); };
    lds_wait(lds0 + L_SYNC + SY_GO);
    if (!lnblk) __builtin_amdgcn_s_sleep(XS0);
    { const unsigned got = gpoll<XSL>(flags + eword(SL_XN, mt * 16 + i16), 0xfffu);
      if (w8 == 0) *(volatile LAS unsigned*)((lds_ptr)smem + L_SYNC + SY_XN) = 1u + ((got >> 12) & 1u); }
    if (w8 == 0) TS(0, 2);
    static_for<36>([&](auto u) { xload(u); });
    u32x4 srdW = mksrd(w1, D * F * 2);
    unsigned so[9];
#pragma unroll
    for (int p = 0; p < 9; ++p) so[p] = (unsigned)((9 * w + p) * 16 * F * 2 - (p & 3) * 1024);
    unsigned voffA = (unsigned)((prow * F + pch * 8 + 288 * cg) * 2) + 64u, voffO = voffA, voffB = voffA;
    PIN(voffO); PIN(voffB);
    unsigned aC = lds0 + L_CNT + lane * 4, one = 1u, tgt = 4u;
    PIN(aC); PIN(one);
    lds_ptr pC = (lds_ptr)smem + L_CNT + lane * 4; PIN(pC);
    const int rdo = (8 * g4 + (i16 >> 2)) * 64 + (g4 & 1) * 32 + (i16 & 3) * 8;
    const lds_ptr rd0 = (lds_ptr)smem + L_G + rdo, rd1 = (lds_ptr)smem + L_G + (rdo ^ 32);
    lds_ptr biasp = (lds_ptr)smem + L_BIAS + g4 * 16;
    lds_wait(lds0 + L_SYNC + SY_ST);
    __builtin_amdgcn_s_setprio(2);
    s16x4 wr[18];
    f32x4 A[4], P[4], Bi[2];
    {
      const lds_ptr rq1 = (lds_ptr)smem + L_Q1 + (8 * g4 + (i16 >> 2)) * 16 + (i16 & 1) * 8;
      auto qread = [&](auto itc) {
        constexpr int it = itc, st = it / 6, tl = (it % 6) >> 1, hf = it & 1;
        if constexpr (st < 18) {
          if constexpr (tl == 0) wr[it % 12] = trread(rd0 + st * 2048 + hf * 256);
          else if constexpr (tl == 1) wr[it % 12] = trread(rd1 + st * 2048 + hf * 256);
          else wr[it % 12] = trread(rq1 + st * 512 + hf * 64);
        }
      };
      static_for<11>([&](auto it) { qread(it); });
      static_for<18>([&](auto ksc) {
        constexpr int ks = ksc;
        waitv<34 - 2 * ks>();
        static_for<6>([&](auto sc) {
          constexpr int sI = sc, tl = sI >> 1, mi = sI & 1, gi = 6 * ks + sI;
          const bf16x8 wf = cat8(wr[(6 * ks + 2 * tl) % 12], wr[(6 * ks + 2 * tl + 1) % 12]);
          f32x4& Dd = tl < 2 ? A[sI & 3] : P[mi];
          if constexpr (ks == 0) mfma_z(Dd, wf, xf[2 * ks + mi]); else mfma_acc(Dd, wf, xf[2 * ks + mi]);
          SBAR();
          qread(std::integral_constant<int, gi + 11>{});
          if constexpr (gi + 11 == 107) arrive<C_R>(aC, one);
          SBAR();
        });
      });
      asm volatile("s_nop 7" : "+v"(A[0]), "+v"(A[1]), "+v"(A[2]), "+v"(A[3]), "+v"(P[0]), "+v"(P[1]));
      const lds_ptr qw = (lds_ptr)smem + L_Q + (32 * w + i16) * QROW + g4 * 8;
#pragma unroll
      for (int i = 0; i < 4; ++i) *(LAS u32x2*)(qw + (i & 1) * 16 * QROW + (i >> 1) * 32) = u32x2{cvtpk(A[i][0] * C2, A[i][1] * C2), cvtpk(A[i][2] * C2, A[i][3] * C2)};
      if (g4 < 2) {
#pragma unroll
        for (int i = 0; i < 2; ++i) *(LAS u32x2*)(qw + i * 16 * QROW + 64) = u32x2{cvtpk(P[i][0] * C2, P[i][1] * C2), cvtpk(P[i][2] * C2, P[i][3] * C2)};
      }
      lds_set(lds0 + L_SYNC + SY_Q + w * 4);
      P[2] = P[0]; P[3] = P[1];
      if (w8 == 0) TS(0, 3);
      lds_wait_ge(lds0 + L_SYNC + SY_ST2, 4u);
    }
    const unsigned vA2 = (unsigned)((((lane >> 2) & 3) * D + (lane >> 4) * 32 + (lane & 3) * 8 + 144 * nq) * 2);
    const unsigned vO2 = (unsigned)((((lane >> 2) & 3) * D + (lane >> 4) * 32 + ((lane & 3) ^ 2) * 8 + 144 * nq) * 2);
    unsigned vB2 = (unsigned)(((lane >> 1) * D + 128 + (lane & 1) * 8 + 144 * nq) * 2);
    auto kpos = [&](int i) { int u = (i < 2 ? i + 7 : (i < 9 ? i - 2 : i)) + 9 * nq; return u >= 36 ? u - 36 : u; };
    const unsigned tabD = lane < 9 ? (unsigned)((1152 * kq + 32 * kpos(4 * lane + w)) * D * 2) : (unsigned)((144 * kq + 36 * w) * D * 2);
    const unsigned tabH = lane < 36 ? (unsigned)kpos(lane) * 2048u : (unsigned)(lane & 3) * 2048u;
    auto set_g2 = [&](int j) {
      const unsigned r = __builtin_amdgcn_readlane(tabD, j);
      voffA = vA2 + r; voffO = vO2 + r; voffB = vB2 + r;
    };
    auto bias_read = [&]() { Bi[0] = *(const LAS f32x4*)biasp; Bi[1] = *(const LAS f32x4*)(biasp + 64); biasp += 128; };
    const __amdgpu_buffer_rsrc_t rsh = MKRS(WS_HF + (unsigned)(mt * 288 + 18 * cg) * 1024, 18 * 1024);
    unsigned hst = voff + 16384u;
    unsigned ep[8] = {}, linc = 0u;
    int sl_cur = GSLOT, sl_dma = 0;
    lds_ptr ra = rd0 + GSLOT, rb1 = rd1 + GSLOT;
    auto wread1 = [&](auto itc) { constexpr int it = itc, st = (it >> 2) % 18; wr[it % 12] = trread(((it & 2) ? rb1 : ra) + st * 2048 + (it & 1) * 256); };
    auto dma_piece = [&](auto pc) { constexpr int p = pc; bdma_m0<(p & 3) * 1024>(p == 8 ? voffB : ((p & 1) ? voffO : voffA), srdW, so[p]); };
    auto pin_so = [&]() {
#pragma unroll
      for (int p = 0; p < 9; ++p) PINS(so[p]);
    };
    bias_read();
    static_for<11>([&](auto it) { wread1(it); });
#pragma unroll 1
    for (int t = 0; t < 9; ++t) {
      voffA += 64; voffO += 64; voffB += 64;
      if (__builtin_expect(t >= 7, 0)) {
        srdW = mksrd(w2, F * D * 2);
#pragma unroll
        for (int p = 0; p < 9; ++p) so[p] = p < 8 ? (unsigned)(p * 4 * D * 2 - (p & 3) * 1024) : 0u;
        set_g2(t - 7);
        __builtin_amdgcn_s_setprio(0);
      }
      pin_so();
      const unsigned ldsd = ldsw + sl_dma;
      const int sl_nxt = sl_cur == 2 * GSLOT ? 0 : sl_cur + GSLOT;
      unsigned cv;
      static_for<72>([&](auto gc) {
        constexpr int gi = gc, ks = gi >> 2, tl = (gi >> 1) & 1, mi = gi & 1;
        if constexpr ((gi & 3) == 0) asm volatile("" :: "v"(wr[gi % 12 + 3]));
        const bf16x8 wf = cat8(wr[(4 * ks + 2 * tl) % 12], wr[(4 * ks + 2 * tl + 1) % 12]);
        if constexpr (ks == 0) mfma_dc(A[gi & 3], wf, xf[2 * ks + mi], Bi[tl]);
        else if constexpr (ks == 17) mfma_dc(P[gi & 3], wf, xf[2 * ks + mi], A[gi & 3]);
        else mfma_acc(A[gi & 3], wf, xf[2 * ks + mi]);
        SBAR();
        if constexpr (gi == 11) { check<C_R>(cv, aC, tgt); tgt += 4u; }
        if constexpr (gi == 61) { check<C_L>(cv, aC, tgt); ra = rd0 + sl_nxt; rb1 = rd1 + sl_nxt; }
        if constexpr (gi == 12 || gi == 40 || gi == 68) set_m0<(gi - 12) / 28 * 4096>(ldsd);
        wread1(std::integral_constant<int, gi + 11>{});
        if constexpr (gi == 0) cv = *(volatile LAS unsigned*)(pC + C_R);
        if constexpr (gi == 52) cv = *(volatile LAS unsigned*)(pC + C_L);
        if constexpr (gi == 60) arrive<C_R>(aC, one);
        if constexpr (gi >= 12 && (gi - 12) % 7 == 0) dma_piece(std::integral_constant<int, (gi - 12) / 7>{});
        if constexpr (gi >= 6 && gi <= 34 && gi % 4 == 2) {
          constexpr int j = (gi - 6) / 4, pi = ((j >> 1) & 1) * 2 + (j >> 2);
          ep[j] = relu2pk(P[pi][2 * (j & 1)], P[pi][2 * (j & 1) + 1]);
        }
        if constexpr (gi == 23 || gi == 37) {
          constexpr int hh = gi == 37;
          __builtin_amdgcn_raw_buffer_store_b128(u32x4{ep[4 * hh], ep[4 * hh + 1], ep[4 * hh + 2], ep[4 * hh + 3]}, rsh, hst + hh * 1024, 0, 16);
        }
        if constexpr (gi == 46) { waitv<7>(); arrive<C_L>(aC, linc); linc = 1u; PIN(linc); hst = voff + (unsigned)t * 2048u; PIN(hst); }
        if constexpr (gi == 44) bias_read();
        SBAR();
      });
      sl_dma = sl_cur; sl_cur = sl_nxt;
    }
    u32x4 hf[8];
    u32x4 srdH = mksrd(ws + WS_HF + ((size_t)mt * 288 + 72 * kq) * 1024, 72 * 1024);
    u32x4 srdX = mksrd(ws + WS_OX + (size_t)(mt * 16 + 4 * kq) * 256, 0);
    unsigned sh[4];
    auto set_h = [&](int j) {
#pragma unroll
      for (int i = 0; i < 4; ++i) sh[i] = __builtin_amdgcn_readlane(tabH, 4 * j + i);
    };
    set_h(0);
    asm volatile("s_nop 4" : "+s"(sh[2]), "+s"(sh[3]));
    static_for<4>([&](auto i) { bload16_sc1<(i & 1) * 1024>(hf[4 + i], voff, srdH, sh[2 + (i >> 1)]); });
    hf[0] = u32x4{ep[0], ep[1], ep[2], ep[3]}; hf[1] = u32x4{ep[4], ep[5], ep[6], ep[7]};
    const int r2o = g4 * 1024 + (i16 >> 2) * 64 + (g4 & 1) * 32 + (i16 & 3) * 8;
    const lds_ptr r2a0 = (lds_ptr)smem + L_G + r2o, r2b0 = (lds_ptr)smem + L_G + (r2o ^ 32);
    const lds_ptr r2h0 = (lds_ptr)smem + L_G + 8192 + (4 * g4 + (i16 >> 2)) * 32 + (i16 & 3) * 8;
    lds_ptr r2a = r2a0 + sl_cur, r2b = r2b0 + sl_cur, r2h = r2h0 + sl_cur;
    auto wread2 = [&](auto itc) {
      constexpr int it = itc, ks = (it / 18) & 3, nt = (it % 18) >> 1, hi = it & 1;
      if constexpr (nt < 8) wr[it % 18] = trread(((nt & 1) ? r2b : r2a) + ks * 9216 + hi * 4096 + (nt >> 1) * 256);
      else wr[it % 18] = trread(r2h + ks * 9216 + hi * 512);
    };
    static_for<17>([&](auto it) { wread2(it); });
    asm volatile("s_nop 7" : "+v"(P[0]), "+v"(P[1]), "+v"(P[2]), "+v"(P[3]));
#pragma unroll
    for (int j = 0; j < 8; ++j) { const int pi = ((j >> 1) & 1) * 2 + (j >> 2); ep[j] = relu2pk(P[pi][2 * (j & 1)], P[pi][2 * (j & 1) + 1]); }
    hf[2] = u32x4{ep[0], ep[1], ep[2], ep[3]}; hf[3] = u32x4{ep[4], ep[5], ep[6], ep[7]};
    __builtin_amdgcn_raw_buffer_store_b128(hf[2], rsh, hst, 0, 16);
    __builtin_amdgcn_raw_buffer_store_b128(hf[3], rsh, hst + 1024, 0, 16);
    f32x4 acc[18] = {};
    const unsigned voffX = (unsigned)(g4 * 256 + i16 * 8);
    lds_wait(lds0 + L_SYNC + SY_KV);
    gu32* fline = flags + (mt * 4 + kq) * 32;
    const unsigned foff = (unsigned)i16 * 4u;
    unsigned fl = 0u;
    const unsigned tabN = lane == 8 ? (0xf0u | (0x100u << nq)) : (((lane & 1) && lane < 6) ? 1u << ((nq + ((lane + 1) >> 1)) & 3) : 0u);
    u32x2 ox0, ox1;
#pragma unroll 1
    for (int j = 0; j < 10; ++j) {
      const int sl_nxt = sl_cur == 2 * GSLOT ? 0 : sl_cur + GSLOT;
      int nD = sl_nxt, nH = sl_nxt;
      unsigned need = __builtin_amdgcn_readlane(tabN, j);
      if (__builtin_expect(j >= 7, 0)) {
        if (j == 7) {
          srdW = mksrd(wo, D * D * 2);
#pragma unroll
          for (int p = 4; p < 8; ++p) so[p] += 2 * D * 2;
          vB2 += (unsigned)(h * 2 * D * 2);
        } else if (j == 8) {
          srdW[2] = 0u;
          srdH = mksrd(ws + WS_OF + ((size_t)mt * 32 + 8 * kq) * 1024, 8 * 1024);
        } else {
          srdH[2] = 0u; srdX[2] = 1024u;
          nD = L_EX - L_G; nH = L_EX + 4096 - L_G - 8192;
        }
      }
      pin_so();
      set_g2(j + 2); set_h(j + 1);
      const unsigned ldsd = ldsw + sl_dma;
      unsigned cv;
      static_for<72>([&](auto gc) {
        constexpr int gi = gc, ks = gi / 18, nt = (gi % 18) >> 1, mi = gi & 1;
        if constexpr (gi % 18 == 0) { waitv<(ks == 0 ? 14 : (ks == 1 ? 15 : (ks == 2 ? 13 : 16)))>(); if constexpr (ks == 2) arrive<C_L>(aC, one); }
        if constexpr (gi % 6 == 0) asm volatile("" :: "v"(wr[gi % 18 + 5]));
        const bf16x8 wf = cat8(wr[2 * nt], wr[2 * nt + 1]);
        mfma_acc(acc[gi % 18], wf, BC(hf[2 * ks + mi]));
        SBAR();
        if constexpr (gi == 5) { check<C_R>(cv, aC, tgt); tgt += 4u; }
        if constexpr (gi == 55) { check<C_L>(cv, aC, tgt); r2a = r2a0 + nD; r2b = r2b0 + nD; r2h = r2h0 + nH; }
        constexpr int dp = gi == 6 ? 0 : gi == 11 ? 1 : gi == 21 ? 2 : gi == 26 ? 3 : gi == 31 ? 4 : gi == 40 ? 5 : gi == 46 ? 6 : gi == 58 ? 7 : gi == 64 ? 8 : -1;
        if constexpr (dp >= 0 && (dp & 3) == 0) set_m0<dp * 1024>(ldsd);
        wread2(std::integral_constant<int, gi + 17>{});
        if constexpr (gi == 0) cv = *(volatile LAS unsigned*)(pC + C_R);
        if constexpr (gi == 44) cv = *(volatile LAS unsigned*)(pC + C_L);
        if constexpr (gi == 54) arrive<C_R>(aC, one);
        if constexpr (gi == 2) bload8_sc1<0>(ox0, voffX, srdX);
        if constexpr (gi == 3) bload8_sc1<128>(ox1, voffX, srdX);
        if constexpr (dp >= 0) dma_piece(std::integral_constant<int, (dp >= 0 ? dp : 0)>{});
        if constexpr (gi == 15) {
          if (need) {
            waitv<10>(); PIN(fl);
            if (((unsigned)__ballot(fl != 0u) & need) != need) { (void)gpoll<1>(fline + i16, need); }
          }
        }
        if constexpr (gi == 49) asm volatile("global_load_dword %0, %1, %2 sc1" : "+v"(fl) : "v"(foff), "s"(fline) : "memory");
        if constexpr (gi % 18 >= 16) bload16_sc1<mi * 1024>(hf[2 * ks + mi], voff, srdH, sh[ks]);
        if constexpr (gi == 24) { if (j == 0) { waitv<7>(); gsignal(fline + nq, lane == 0); } }
        SBAR();
      });
      sl_dma = sl_cur; sl_cur = sl_nxt;
    }
    waitv<0>();
    asm volatile("s_nop 7" : "+v"(ox0), "+v"(ox1) : "v"(fl), "v"(hf[0]), "v"(hf[1]), "v"(hf[2]), "v"(hf[3]), "v"(hf[4]), "v"(hf[5]), "v"(hf[6]), "v"(hf[7]));
#pragma unroll
    for (int i = 0; i < 18; ++i) acc[i] = __builtin_amdgcn_mfma_f32_16x16x16bf16_1k(wr[i & ~1], __builtin_bit_cast(s16x4, (i & 1) ? ox1 : ox0), acc[i], 0, 0, 0);
    if (w8 == 0) TS(0, 4);
    if (w != kq) {
      const __amdgpu_buffer_rsrc_t rsp = MKRS(WS_PART + (unsigned)((mt * 4 + nq) * 4 + kq) * 9 * 1024, 9 * 1024);
#pragma unroll
      for (int nt = 0; nt < 9; ++nt) {
        auto r0 = __builtin_amdgcn_permlane16_swap(cvtpk(acc[2 * nt][0], acc[2 * nt][1]), cvtpk(acc[2 * nt + 1][0], acc[2 * nt + 1][1]), false, false);
        auto r1 = __builtin_amdgcn_permlane16_swap(cvtpk(acc[2 * nt][2], acc[2 * nt][3]), cvtpk(acc[2 * nt + 1][2], acc[2 * nt + 1][3]), false, false);
        __builtin_amdgcn_raw_buffer_store_b128(u32x4{r0[0], r1[0], r0[1], r1[1]}, rsp, voff + nt * 1024, 0, 16);
      }
    } else {
#pragma unroll
      for (int gi = 0; gi < 18; ++gi) *(LAS f32x4*)((lds_ptr)smem + L_KV + gi * 1024 + voff) = acc[gi];
      lds_set(lds0 + L_SYNC + SY_F);
    }
  } else {
    __builtin_amdgcn_s_setprio(1);
    const char* kv = ws + WS_KV;
    const unsigned ldsk = lds0 + L_KV + 5 * w * 1024;
    const char* kvsrc = kv + REC + 5 * w * 1024;
    lds_wait(lds0 + L_SYNC + SY_KV);
    bf16x8 kA0, kB0;
    unsigned k1t;
    u32x2 kA1 = {0u, 0x3f803f80u}, kB1 = {0u, 0x3f803f80u};
    asm volatile("global_load_dwordx4 %0, %2, %4 sc1\n\tglobal_load_dword %1, %3, %4 offset:1024 sc1" : "=&v"(kA0), "=&v"(k1t) : "v"(voff), "v"((unsigned)m * 4u), "s"(kv) : "memory");
    static_for<4>([&](auto p) { glds16<p * 1024, p * 1024>(voff, kvsrc, ldsk); });
    glds16<4096, 0>(voff, kvsrc + 4096, ldsk);
    bf16x8 vA0 = {}, vA1 = {}, vB0, vB1;
    f32x16 oa = {}, ob = {};
    u32x4 pa0, pa1, pb0 = {}, pb1 = {};
    const int vr = m < DH ? m : DH;
    lds_ptr kp0 = (lds_ptr)smem + L_KV + lane * 16, kp1 = (lds_ptr)smem + L_KV + 1024 + m * 4, vp = (lds_ptr)smem + L_KV + 1152 + h * 304 + vr * 16;
    unsigned abar_addr = lds0 + L_SYNC + SY_A;
    const LAS unsigned* qrow = (const LAS unsigned*)((lds_ptr)smem + L_Q + (32 * w + m) * QROW + (36 * cg - n0q) * 2 + h * 8);
    PIN(vA0); PIN(vA1); PIN(oa); PIN(ob); PIN(pb0); PIN(pb1); PIN(kp0); PIN(kp1); PIN(vp); PIN(abar_addr); PIN(qrow); PIN(kB1);
    asm volatile("s_waitcnt vmcnt(5)" : "+v"(kA0), "+v"(k1t) :: "memory");
    kA1[0] = k1t; PIN(kA1);
    const unsigned aC = lds0 + L_CNT + lane * 4;
    lds_wait(lds0 + L_SYNC + SY_Q + w * 4);
    u32x4 qa0 = {qrow[0], qrow[1], qrow[4], qrow[5]};
    u32x4 qb0 = {qrow[9], qrow[10], qrow[13], qrow[14]};
    u32x2 qa1 = {qrow[8], 0u}, qb1 = {qrow[17], 0u};
    qa1[0] = h ? 0u : qa1[0]; qb1[0] = h ? 0u : qb1[0];
    f32x16 sa = MFMA8(kA1, qa1); sa = MFMA(kA0, BC(qa0), sa);
    f32x16 sb = MFMA8(kA1, qb1); sb = MFMA(kA0, BC(qb0), sb);
    float ma = __builtin_rintf(tile_max(sa)) + (float)M_OFF, mb = __builtin_rintf(tile_max(sb)) + (float)M_OFF;
    const bool bad0 = !(ma > -M_MAX) || !(mb > -M_MAX);
    set_negm(qa1, ma, h); set_negm(qb1, mb, h);
#pragma unroll
    for (int r = 0; r < 16; ++r) sa[r] -= ma;
    unsigned pfj = 0u;
    const __amdgpu_buffer_rsrc_t rsp = MKRS(WS_PART + (unsigned)(((rb * 4 + kq) * 4 + nq) * 4) * 9 * 1024, 4 * 9 * 1024);
#define EX(v) __builtin_amdgcn_exp2f(v)
#define GAP(MF, SS, PW, i) do { MF; SBAR(); { const float e0 = EX(SS[4 * (i)]), e1 = EX(SS[4 * (i) + 1]), e2 = EX(SS[4 * (i) + 2]), e3 = EX(SS[4 * (i) + 3]); \
      PW[(2 * (i)) & 3] = cvtpk(e0, e1); PW[(2 * (i) + 1) & 3] = cvtpk(e2, e3); } PIN(PW); SBAR(); } while (0)
#define RESC(FL, O, SS, M, Q1) do { if (__builtin_expect(FL, 0)) { \
      int e = __builtin_amdgcn_frexp_expf(O[10]) + M_OFF; e = e > 0 ? e : 0; const float ef = (float)e; \
      _Pragma("unroll") for (int r = 0; r < 16; ++r) { O[r] = __builtin_amdgcn_ldexpf(O[r], -e); SS[r] -= ef; } \
      M += ef; set_negm(Q1, M, h); } SBAR(); } while (0)
#define ITER(tt, CK, KC0, KC1, VC0, VC1, KN0, KN1, VN0, VN1) do { \
    bool flb = false, fla = false; \
    GAP(sb = MFMA8(KC1, qb1); SBAR(); KN1[0] = *(const LAS unsigned*)(kp1 + (tt) * REC); KN0 = *(const LAS bf16x8*)(kp0 + (tt) * REC); if (CK) flb = __any(!(ob[10] <= L_TRIG)), sa, pa0, 0); \
    GAP(sb = MFMA(KC0, BC(qb0), sb); SBAR(); VN0 = *(const LAS bf16x8*)(vp + (tt) * REC); VN1 = *(const LAS bf16x8*)(vp + (tt) * REC + 608), sa, pa0, 1); \
    GAP(ob = MFMA(VC0, BC(pb0), ob), sa, pa1, 2); \
    GAP(ob = MFMA(VC1, BC(pb1), ob), sa, pa1, 3); \
    if (CK) RESC(flb, ob, sb, mb, qb1); \
    asm volatile("" :: "v"(VN1)); \
    GAP(sa = MFMA8(KN1, qa1); SBAR(); if (CK) fla = __any(!(oa[10] <= L_TRIG)), sb, pb0, 0); \
    GAP(sa = MFMA(KN0, BC(qa0), sa), sb, pb0, 1); \
    GAP(oa = MFMA(VN0, BC(pa0), oa), sb, pb1, 2); \
    GAP(oa = MFMA(VN1, BC(pa1), oa), sb, pb1, 3); \
    if (CK) RESC(fla, oa, sa, ma, qa1); \
  } while (0)
#pragma unroll 1
    for (int c = 0; c < 8; ++c) {
      {
        if (c >= 4 && c <= 6 && w != kq) swbar<3>(abar_addr, 4 * (c + 1)); else swbar<0>(abar_addr, 4 * (c + 1));
        if (w8 == 4 && c == 0) TS(2, 0);
        if (c < 7) {
          const char* src = kvsrc + (size_t)(c + 1) * CHUNK; const unsigned dst = ldsk + ((c + 1) & 1) * CHUNK;
          static_for<4>([&](auto p) { glds16<p * 1024, p * 1024>(voff, src, dst); });
          glds16<4096, 0>(voff, src + 4096, dst);
        }
        if (c < 3) {
          const u32x4 s2 = mksrd(w2 + (size_t)1152 * kq * D, 1152 * D * 2);
          const unsigned li = lane < 27 ? (unsigned)(((c * 32 + bidx) * 4 + w) * 27 + lane) * 128u : 0x7fffffffu;
          asm volatile("buffer_load_dword %0, %1, %2, 0 offen" : "+v"(pfj) : "v"(li), "s"(s2) : "memory");
        }
        if (c == 2) {
          const u32x4 s2 = mksrd(wo + 144 * kq * D, 144 * D * 2);
          const unsigned li = lane < 11 ? (unsigned)((bidx * 4 + w) * 11 + lane) * 128u : 0x7fffffffu;
          asm volatile("buffer_load_dword %0, %1, %2, 0 offen" : "+v"(pfj) : "v"(li), "s"(s2) : "memory");
        }
        if (c >= 3 && c <= 5) {
          if (w != kq) {
#pragma unroll
            for (int i = 0; i < 3; ++i) __builtin_amdgcn_raw_buffer_store_b128(u32x4{~0u, ~0u, ~0u, ~0u}, rsp, voff + (unsigned)(w * 9216 + (c - 3) * 3072) + i * 1024, 0, 16);
          }
        } else if (c == 6) {
          const int r = (lane >> 2) & 3;
          const int grow = 36 * (4 * kq + w) + 16 + (r >> 1) * 18 + (r & 1);
          glds16<L_EX, 0>((unsigned)((grow * D + 144 * nq + (lane >> 4) * 32 + ((lane & 3) ^ ((w & 1) << 1)) * 8) * 2), wo, lds0 + w * 1024);
          const int rho = (lane >> 1) & 15;
          const int grow2 = 36 * (4 * kq + (rho >> 2)) + 16 + ((rho >> 1) & 1) * 18 + (rho & 1);
          glds16<L_EX + 4096, 0>((unsigned)((grow2 * D + 144 * nq + 128 + (lane & 1) * 8) * 2), wo, lds0);
        } else if (c == 7) gsignal(flags + ((rb * 4 + kq) * 4 + (lane & 3)) * 32 + 8 + nq, w == 0 && lane < 4);
      }
#pragma unroll
      for (int t2 = 0; t2 < 4; ++t2) {
        ITER(2 * t2, false, kA0, kA1, vA0, vA1, kB0, kB1, vB0, vB1);
        ITER(2 * t2 + 1, true, kB0, kB1, vB0, vB1, kA0, kA1, vA0, vA1);
      }
      PIN(pfj);
      const int dlt = (c & 1) ? -CHUNK : CHUNK;
      kp0 += dlt; kp1 += dlt; vp += dlt;
    }
    ob = MFMA(vA0, BC(pb0), ob); ob = MFMA(vA1, BC(pb1), ob);
#undef ITER
#undef RESC
#undef GAP
#undef EX
    {
      float ca = 0.f, cb = 0.f;
#pragma unroll
      for (int r = 0; r < 11; ++r) { ca = __builtin_fmaf(oa[r], 0.f, ca); cb = __builtin_fmaf(ob[r], 0.f, cb); }
      const bool bad = bad0 || !(ca == 0.f) || !(cb == 0.f) || !(oa[10] > L_MIN) || !(ob[10] > L_MIN) || !(oa[10] < L_MAX) || !(ob[10] < L_MAX) || !(ma < M_MAX) || !(mb < M_MAX);
      if (__builtin_expect(__any(bad), 0)) { oa = attn_slow(kv, BC(qa0), qa1, lane); ob = attn_slow(kv, BC(qb0), qb1, lane); }
    }
    {
      const float ra = 1.0f / oa[10], rbb = 1.0f / ob[10];
      const u32x4 f0 = {cvtpk(oa[0] * ra, oa[1] * ra), cvtpk(oa[2] * ra, oa[3] * ra), cvtpk(ob[0] * rbb, ob[1] * rbb), cvtpk(ob[2] * rbb, ob[3] * rbb)};
      const u32x4 f1 = {cvtpk(oa[4] * ra, oa[5] * ra), cvtpk(oa[6] * ra, oa[7] * ra), cvtpk(ob[4] * rbb, ob[5] * rbb), cvtpk(ob[6] * rbb, ob[7] * rbb)};
      const __amdgpu_buffer_rsrc_t rso = MKRS(WS_OF + (unsigned)(mt * 32 + 2 * cg) * 1024, 2048), rsx = MKRS(WS_OX + (unsigned)(mt * 16 + cg) * 256, 256);
      const unsigned oo = (unsigned)((m >> 4) * 1024 + ((m & 15) + 16 * h) * 16);
      __builtin_amdgcn_raw_buffer_store_b128(f0, rso, oo, 0, 16);
      __builtin_amdgcn_raw_buffer_store_b128(f1, rso, oo + 512, 0, 16);
      const u32x2 ex = {cvtpk(oa[8] * ra, oa[9] * ra), cvtpk(ob[8] * rbb, ob[9] * rbb)};
      if (h == 0) __builtin_amdgcn_raw_buffer_store_b64(ex, rsx, m * 8, 0, 16);
      waitv<0>();
      gsignal(flags + (mt * 4 + kq) * 32 + 4 + nq, lane == 0);
    }
    if (w8 == 4) TS(2, 1);
    __builtin_amdgcn_s_setprio(0);
    {
      const int fm = rb * 4 + kq;
      const int p0 = w == 3 ? 6 : 2 * w, np = w == 3 ? 3 : 2;
      const int g4 = lane >> 4;
      const size_t rowoff = (size_t)(32 * fm + 16 * (g4 & 1) + (lane & 15)) * D + 144 * nq;
      u32x4 xv[3], bv[3];
#pragma unroll
      for (int i = 0; i < 3; ++i) { const int pi = i < np ? p0 + i : p0; xv[i] = *(const u32x4*)(x + rowoff + 16 * pi + 8 * h); bv[i] = *(const u32x4*)(b2 + 144 * nq + 16 * pi + 8 * h); }
      lds_wait(lds0 + L_SYNC + SY_F);
      u32x4 t[3][3];
      auto pload = [&]() {
#pragma unroll
        for (int q = 0; q < 3; ++q) {
          const int src = q < kq ? q : q + 1;
#pragma unroll
          for (int i = 0; i < 3; ++i) { const int pi = i < np ? p0 + i : p0; t[q][i] = __builtin_amdgcn_raw_buffer_load_b128(rsp, voff, (src * 9 + pi) * 1024, 16); }
        }
      };
      pload();
      SBAR();
      float base[3][8];
#pragma unroll
      for (int i = 0; i < 3; ++i) {
        const int pi = i < np ? p0 + i : p0;
        const f32x4 o0 = *(const LAS f32x4*)((lds_ptr)smem + L_KV + pi * 2048 + voff), o1 = *(const LAS f32x4*)((lds_ptr)smem + L_KV + pi * 2048 + 1024 + voff);
#pragma unroll
        for (int e = 0; e < 4; ++e) {
          auto r = __builtin_amdgcn_permlane16_swap(__float_as_uint(o0[e]), __float_as_uint(o1[e]), false, false);
          const unsigned xa = xv[i][e >> 1], ba = bv[i][e >> 1], xb = xv[i][2 + (e >> 1)], bb = bv[i][2 + (e >> 1)];
          base[i][e] = __uint_as_float(r[0]) + ((e & 1) ? bf_hi(xa) + bf_hi(ba) : bf_lo(xa) + bf_lo(ba));
          base[i][4 + e] = __uint_as_float(r[1]) + ((e & 1) ? bf_hi(xb) + bf_hi(bb) : bf_lo(xb) + bf_lo(bb));
        }
      }
      SBAR();
#pragma unroll 1
      for (int it = 0; it < (1 << 14); ++it) {
        unsigned mx = 0u;
#pragma unroll
        for (int q = 0; q < 3; ++q)
#pragma unroll
          for (int i = 0; i < 3; ++i)
#pragma unroll
            for (int e = 0; e < 4; ++e) mx = mx > t[q][i][e] ? mx : t[q][i][e];
        if (!__any(mx == ~0u)) break;
        asm volatile("" ::: "memory");
        pload();
      }
#pragma unroll
      for (int i = 0; i < 3; ++i) {
        u32x4 o;
#pragma unroll
        for (int e = 0; e < 4; ++e)
          o[e] = cvtpk(((base[i][2 * e] + bf_lo(t[0][i][e])) + bf_lo(t[1][i][e])) + bf_lo(t[2][i][e]), ((base[i][2 * e + 1] + bf_hi(t[0][i][e])) + bf_hi(t[1][i][e])) + bf_hi(t[2][i][e]));
        if (i < np) *(u32x4*)(out + rowoff + 16 * (p0 + i) + 8 * h) = o;
      }
      if (w8 == 4) TS(2, 2);
    }
  }
}

extern "C" void kernel_launch(void* const* d_in, const int* in_sizes, int n_in, void* d_out, int out_size, void* d_ws, size_t ws_size, hipStream_t stream) {
  const u16* x = (const u16*)d_in[0];
  const u16* wq = (const u16*)d_in[1];
  const u16* wk = (const u16*)d_in[2];
  const u16* wv = (const u16*)d_in[3];
  const u16* wo = (const u16*)d_in[4];
  const u16* w1 = (const u16*)d_in[6];
  const u16* b1 = (const u16*)d_in[7];
  const u16* w2 = (const u16*)d_in[8];
  const u16* b2 = (const u16*)d_in[9];
  char* ws = (char*)d_ws;
  (void)hipFuncSetAttribute((const void*)k_main, hipFuncAttributeMaxDynamicSharedMemorySize, L_TOTAL);
  k_main<<<256, 512, L_TOTAL, stream>>>(x, wq, wk, wv, wo, w1, b1, w2, b2, ws, (u16*)d_out);
}
```

```cpp
#include <hip/hip_runtime.h>
#include <hip/hip_bf16.h>
#include <stdint.h>
#include <type_traits>

typedef unsigned short u16;
using bf16x8 = __attribute__((ext_vector_type(8))) short;
using s16x4 = __attribute__((ext_vector_type(4))) short;
using f32x16 = __attribute__((ext_vector_type(16))) float;
using f32x4 = __attribute__((ext_vector_type(4))) float;
using u32x4 = __attribute__((ext_vector_type(4))) unsigned;
using u32x2 = __attribute__((ext_vector_type(2))) unsigned;
typedef float f32x2_t __attribute__((ext_vector_type(2)));
typedef __bf16 bf16x2_t __attribute__((ext_vector_type(2)));
#define LAS __attribute__((address_space(3)))
#define GAS __attribute__((address_space(1)))
typedef LAS char* lds_ptr;
typedef GAS unsigned gu32;
typedef short v4i16_t __attribute__((ext_vector_type(4)));

#define DI __device__ __forceinline__
#define SBAR() __builtin_amdgcn_sched_barrier(0)
#define PIN(x) asm volatile("" : "+v"(x))
#define PINS(x) asm volatile("" : "+s"(x))
#define MFMA(a, b, c) __builtin_amdgcn_mfma_f32_32x32x16_bf16((a), (b), (c), 0, 0, 0)
#define BC(x) __builtin_bit_cast(bf16x8, x)
#define RLX_AGENT __ATOMIC_RELAXED, __HIP_MEMORY_SCOPE_AGENT

constexpr int D = 576, DH = 18, F = 4608;
constexpr float C2 = 0.23570226039551584f * 1.4426950408889634f;

constexpr unsigned WS_FLAGS = 0;
constexpr unsigned FL_E = 256 * 128;
constexpr int SL_XN = 0;
constexpr int SL_KV = 64;
constexpr int SL_ACK = 72;
constexpr unsigned FL_BEAT = 65536;
constexpr unsigned eword(int slot0, int i) { return (FL_E + (unsigned)(slot0 + (i >> 4)) * 256u) / 4u + (unsigned)(i & 15); }
constexpr unsigned WS_XNF = 131072;
constexpr unsigned WS_KV = WS_XNF + 64 * 36 * 1024;
constexpr unsigned WS_OF = WS_KV + 196608;
constexpr unsigned WS_OX = WS_OF + 64 * 32 * 1024;
constexpr unsigned WS_HF = WS_OX + 64 * 16 * 256;
constexpr unsigned WS_PART = WS_HF + 64 * 288 * 1024;

constexpr int REC = 2560, CHUNK = 8 * REC;
constexpr int GSLOT = 36864;
constexpr int L_EX = 0, L_BIAS = 5120, L_SYNC = L_BIAS + 1152, L_G = 6400, L_KV = L_G + 3 * GSLOT, L_CNT = L_KV + 2 * CHUNK  , L_TOTAL = L_CNT + 512;
constexpr int L_Q = L_KV + CHUNK, QROW = 88;
constexpr int L_Q1 = L_Q + 128 * QROW;
constexpr int SY_LN = 0, SY_A = 4, SY_Q = 8  , SY_F = 24, SY_GO = 28, SY_KVW = 32, SY_KV = 36, SY_ST = 40, SY_Z = 44, SY_ST2 = 48, SY_XN = 52, SY_KD = 56;
constexpr int L_LN = L_G + 2 * GSLOT;

DI unsigned cvtpk(float lo, float hi) { f32x2_t v = {lo, hi}; bf16x2_t b = __builtin_convertvector(v, bf16x2_t); return __builtin_bit_cast(unsigned, b); }
DI float bf_lo(unsigned u) { return __uint_as_float(u << 16); }
DI float bf_hi(unsigned u) { return __uint_as_float(u & 0xffff0000u); }
DI float bfr(float x) { return bf_lo(cvtpk(x, x) & 0xffffu); }
DI s16x4 trread(lds_ptr p) { return __builtin_bit_cast(s16x4, __builtin_amdgcn_ds_read_tr16_b64_v4i16((LAS v4i16_t*)p)); }
DI bf16x8 cat8(s16x4 a, s16x4 b) { return __builtin_shufflevector(a, b, 0, 1, 2, 3, 4, 5, 6, 7); }
template <int LI, int GI>
DI void glds16(unsigned voff, const void* sbase, unsigned ldsw) {
  asm volatile("s_add_u32 m0, %2, %3\n\ts_nop 0\n\tglobal_load_lds_dwordx4 %0, %1 offset:%4 sc1"
               :: "v"(voff), "s"(sbase), "s"(ldsw), "i"(LI - GI), "i"(GI) : "memory", "scc");
}
DI u32x4 mksrd(const void* p, unsigned bytes) { const unsigned long long a = (unsigned long long)p; return u32x4{(unsigned)a, (unsigned)(a >> 32) & 0xffffu, bytes, 0x00020000u}; }
template <int LI>
DI void bdma16(unsigned voff, u32x4 srd, unsigned soff, unsigned ldsw) {
  asm volatile("s_add_u32 m0, %3, %4\n\ts_nop 0\n\tbuffer_load_dwordx4 %0, %1, %2 offen lds"
               :: "v"(voff), "s"(srd), "s"(soff), "s"(ldsw), "i"(LI) : "memory", "scc");
}
template <int OFF>
DI void bload16_sc1(u32x4& dst, unsigned voff, u32x4 srd, unsigned soff) {
  asm volatile("buffer_load_dwordx4 %0, %1, %2, %3 offen offset:%4 sc1" : "=v"(dst) : "v"(voff), "s"(srd), "s"(soff), "i"(OFF) : "memory");
}
template <int OFF>
DI void gload16(bf16x8& dst, unsigned voff, const void* sbase) { asm volatile("global_load_dwordx4 %0, %1, %2 offset:%3 sc1" : "=v"(dst) : "v"(voff), "s"(sbase), "i"(OFF) : "memory"); }
template <int N> DI void waitv() { asm volatile("s_waitcnt vmcnt(%c0)" :: "i"(N) : "memory"); }
template <int OFF> DI void arrive(unsigned addr, unsigned one) { asm volatile("ds_add_u32 %0, %1 offset:%2" :: "v"(addr), "v"(one), "i"(OFF) : "memory"); }
template <int OFF> DI void check(unsigned v, unsigned addr, unsigned target) {
  asm volatile(
      "v_cmp_gt_u32 vcc, %2, %0\n\t"
      "s_cbranch_vccz 2f\n\t"
      "1:\n\t"
      "ds_read_b32 %0, %1 offset:%3\n\t"
      "s_waitcnt lgkmcnt(0)\n\t"
      "v_cmp_gt_u32 vcc, %2, %0\n\t"
      "s_cbranch_vccnz 1b\n\t"
      "2:"
      : "+v"(v) : "v"(addr), "s"(target), "i"(OFF) : "memory", "vcc");
}
constexpr int C_R = 0, C_L = 256;
template <int LI> DI void set_m0(unsigned ldsw) { asm volatile("s_add_u32 m0, %0, %1" :: "s"(ldsw), "i"(LI) : "memory", "scc"); }
template <int OFF> DI void bdma_m0(unsigned voff, u32x4 srd, unsigned soff) {
  asm volatile("buffer_load_dwordx4 %0, %1, %2 offen offset:%3 lds" :: "v"(voff), "s"(srd), "s"(soff), "i"(OFF) : "memory");
}


template <int VM>
DI void swbar(unsigned addr, unsigned target) {
  unsigned v, s;
  asm volatile(
      "s_waitcnt vmcnt(%c4) lgkmcnt(0)\n\t"
      "s_mov_b64 exec, 1\n\t"
      "v_mov_b32 %0, 1\n\t"
      "ds_add_rtn_u32 %0, %2, %0\n\t"
      "s_waitcnt lgkmcnt(0)\n\t"
      "v_add_u32 %0, 1, %0\n\t"
      "1:\n\t"
      "s_nop 0\n\t"
      "v_readfirstlane_b32 %1, %0\n\t"
      "s_nop 1\n\t"
      "s_cmp_ge_u32 %1, %3\n\t"
      "s_cbranch_scc1 2f\n\t"
      "ds_read_b32 %0, %2\n\t"
      "s_waitcnt lgkmcnt(0)\n\t"
      "s_branch 1b\n\t"
      "2:\n\t"
      "s_mov_b64 exec, -1"
      : "=&v"(v), "=&s"(s) : "v"(addr), "s"(target), "i"(VM) : "memory", "scc");
}
DI void lds_wait(unsigned addr) {
  unsigned v, s;
  asm volatile(
      "1:\n\t"
      "ds_read_b32 %0, %2\n\t"
      "s_waitcnt lgkmcnt(0)\n\t"
      "v_readfirstlane_b32 %1, %0\n\t"
      "s_nop 1\n\t"
      "s_cmp_lg_u32 %1, 0\n\t"
      "s_cbranch_scc1 2f\n\t"
      "s_sleep 2\n\t"
      "s_branch 1b\n\t"
      "2:"
      : "=&v"(v), "=&s"(s) : "v"(addr) : "memory", "scc");
}
DI unsigned lds_inc(unsigned addr) {
  unsigned v;
  asm volatile("s_waitcnt vmcnt(0) lgkmcnt(0)\n\ts_mov_b64 exec, 1\n\tv_mov_b32 %0, 1\n\tds_add_rtn_u32 %0, %1, %0\n\ts_waitcnt lgkmcnt(0)\n\ts_mov_b64 exec, -1" : "=&v"(v) : "v"(addr) : "memory");
  return __builtin_amdgcn_readfirstlane(v);
}
DI unsigned lds_inc_nw(unsigned addr) {
  unsigned v;
  asm volatile("s_waitcnt lgkmcnt(0)\n\ts_mov_b64 exec, 1\n\tv_mov_b32 %0, 1\n\tds_add_rtn_u32 %0, %1, %0\n\ts_waitcnt lgkmcnt(0)\n\ts_mov_b64 exec, -1" : "=&v"(v) : "v"(addr) : "memory");
  return __builtin_amdgcn_readfirstlane(v);
}
DI void lds_wait_ge(unsigned addr, unsigned target) {
  unsigned v, s;
  asm volatile(
      "1:\n\t"
      "ds_read_b32 %0, %2\n\t"
      "s_waitcnt lgkmcnt(0)\n\t"
      "v_readfirstlane_b32 %1, %0\n\t"
      "s_nop 1\n\t"
      "s_cmp_ge_u32 %1, %3\n\t"
      "s_cbranch_scc1 2f\n\t"
      "s_sleep 2\n\t"
      "s_branch 1b\n\t"
      "2:"
      : "=&v"(v), "=&s"(s) : "v"(addr), "s"(target) : "memory", "scc");
}
DI void lds_set(unsigned addr) {
  unsigned v;
  asm volatile("s_waitcnt lgkmcnt(0)\n\tv_mov_b32 %0, 1\n\tds_write_b32 %1, %0" : "=&v"(v) : "v"(addr) : "memory");
}
template <int SL>
DI unsigned gpoll(gu32* p, unsigned need) {
  unsigned got = 0u;
#pragma unroll 1
  for (unsigned i = 0; i < (1u << 18); ++i) {
    got = (unsigned)__ballot(__hip_atomic_load(p, RLX_AGENT) != 0u);
    if ((got & need) == need) break;
    __builtin_amdgcn_s_sleep(SL);
  }
  __builtin_amdgcn_fence(__ATOMIC_ACQUIRE, "wavefront");
  asm volatile("" ::: "memory");
  return got;
}
DI void gsignal(gu32* p, bool on) { if (on) __hip_atomic_store(p, 1u, RLX_AGENT); }

#define MKRS(off, bytes) __builtin_amdgcn_make_buffer_rsrc((void*)(ws + (off)), 0, (int)(bytes), 0x00020000)
#define M16(a, b, c) __builtin_amdgcn_mfma_f32_16x16x32_bf16((a), (b), (c), 0, 0, 0)
DI float xg_sum(float v) {
  auto r = __builtin_amdgcn_permlane16_swap(__float_as_uint(v), __float_as_uint(v), false, false);
  v = __uint_as_float(r[0]) + __uint_as_float(r[1]);
  auto q = __builtin_amdgcn_permlane32_swap(__float_as_uint(v), __float_as_uint(v), false, false);
  return __uint_as_float(q[0]) + __uint_as_float(q[1]);
}
DI void ln_kv(const u16* __restrict__ x, const u16* __restrict__ wk, const u16* __restrict__ wv, char* __restrict__ ws, lds_ptr smem, int mt, int w, int lane) {
  const int m = lane & 15, g = lane >> 4;
  const unsigned lds0 = (unsigned)(uintptr_t)smem;
  const u16* xrow = x + (size_t)(16 * mt + m) * D;
  u32x4 xr[3];
#pragma unroll
  for (int i = 0; i < 3; ++i) xr[i] = *(const u32x4*)(xrow + 32 * (3 * w + i) + 8 * g);
  unsigned c0u = *(const unsigned*)xrow;
  typedef unsigned u32x4u __attribute__((ext_vector_type(4), aligned(4)));
  u32x4 ra[3], rb[3]; unsigned rc[3];
#pragma unroll
  for (int i = 0; i < 3; ++i) {
    const int r = lane + 64 * i;
    const char* src = (r < 96 ? (const char*)wk + r * 36 : (const char*)wv + (r - 96) * 36) + w * 3456;
    ra[i] = *(const u32x4u*)src; rb[i] = *(const u32x4u*)(src + 16); rc[i] = *(const unsigned*)(src + 32);
  }
  SBAR();
  asm volatile("s_barrier" ::: "memory");
  SBAR();
  asm volatile("" : "+v"(xr[0]), "+v"(xr[1]), "+v"(xr[2]), "+v"(c0u));
  const float c0 = __uint_as_float(c0u << 16);
  const lds_ptr im = smem + L_LN + w * 9216;
  LAS float* stats = (LAS float*)(smem + L_EX);
  float v[24], s = 0.f, q = 0.f;
#pragma unroll
  for (int i = 0; i < 3; ++i)
#pragma unroll
    for (int j = 0; j < 4; ++j) { v[8 * i + 2 * j] = bf_lo(xr[i][j]); v[8 * i + 2 * j + 1] = bf_hi(xr[i][j]); }
#pragma unroll
  for (int i = 0; i < 24; ++i) { const float d = v[i] - c0; s += d; q = __builtin_fmaf(d, d, q); }
  s = xg_sum(s); q = xg_sum(q);
  if (g == 0) *(LAS f32x2_t*)&stats[m * 12 + 2 * w] = f32x2_t{s, q};
  swbar<63>(lds0 + L_SYNC + SY_LN, 6u);
  s = 0.f; q = 0.f;
#pragma unroll
  for (int i = 0; i < 3; ++i) { const f32x4 t = *(const LAS f32x4*)&stats[m * 12 + 4 * i]; s += t[0] + t[2]; q += t[1] + t[3]; }
  const float md = s * (1.f / 576.f);
  const float var = __builtin_fmaxf(__builtin_fmaf(-md, md, q * (1.f / 576.f)), 0.f);
  const float mb = bfr(c0 + md), sd = bfr(__builtin_amdgcn_sqrtf(bfr(bfr(var) + 1e-5f)));
  const float rinv = __builtin_amdgcn_rcpf(sd);
  u32x4 o[3];
  const __amdgpu_buffer_rsrc_t rsx = MKRS(WS_XNF + (unsigned)(mt * 18 + 3 * w) * 1024, 3072);
#pragma unroll
  for (int i = 0; i < 3; ++i) {
#pragma unroll
    for (int j = 0; j < 4; ++j) {
      const unsigned dp = cvtpk(v[8 * i + 2 * j] - mb, v[8 * i + 2 * j + 1] - mb);
      o[i][j] = cvtpk(bf_lo(dp) * rinv, bf_hi(dp) * rinv);
    }
    __builtin_amdgcn_raw_buffer_store_b128(o[i], rsx, i * 1024 + lane * 16, 0, 16);
  }
  SBAR();
#pragma unroll
  for (int i = 0; i < 3; ++i) {
    const lds_ptr dst = im + (lane + 64 * i) * 48;
    *(LAS u32x4*)dst = ra[i]; *(LAS u32x4*)(dst + 16) = rb[i]; *(LAS unsigned*)(dst + 32) = rc[i];
  }
  const lds_ptr rd = im + (8 * g + (m >> 2)) * 48 + (m & 3) * 8, rd1 = im + (8 * g + (m >> 2)) * 48 + 32;
  f32x4 acc[4] = {};
#pragma unroll
  for (int i = 0; i < 3; ++i) {
    acc[0] = M16(cat8(trread(rd + i * 1536), trread(rd + i * 1536 + 192)), BC(o[i]), acc[0]);
    acc[1] = M16(cat8(trread(rd1 + i * 1536), trread(rd1 + i * 1536 + 192)), BC(o[i]), acc[1]);
    acc[2] = M16(BC(o[i]), cat8(trread(rd + 4608 + i * 1536), trread(rd + 4608 + i * 1536 + 192)), acc[2]);
    acc[3] = M16(BC(o[i]), cat8(trread(rd1 + 4608 + i * 1536), trread(rd1 + 4608 + i * 1536 + 192)), acc[3]);
  }
#pragma unroll
  for (int i = 0; i < 4; ++i) *(LAS f32x4*)(im + i * 1024 + lane * 16) = acc[i];
  (void)lds_inc_nw(lds0 + L_SYNC + SY_LN);
  gu32* flags = (gu32*)(ws + WS_FLAGS);
  lds_wait(lds0 + L_SYNC + SY_GO);
  waitv<0>();
  gsignal(flags + eword(SL_XN, (mt >> 1) * 16 + (mt & 1) * 6 + w), lane == 0);
}
DI void kv_out(char* __restrict__ ws, lds_ptr smem, int mt, int lane) {
  const int m = lane & 15, g = lane >> 4;
  const unsigned lds0 = (unsigned)(uintptr_t)smem;
  gu32* flags = (gu32*)(ws + WS_FLAGS);
  lds_wait_ge(lds0 + L_SYNC + SY_LN, 12u);
  u32x2 p[4];
#pragma unroll
  for (int j = 0; j < 4; ++j) {
    f32x4 a = {};
#pragma unroll
    for (int ww = 0; ww < 6; ++ww) a += *(const LAS f32x4*)(smem + L_LN + ww * 9216 + j * 1024 + lane * 16);
    p[j] = u32x2{cvtpk(a[0], a[1]), cvtpk(a[2], a[3])};
  }
  lds_set(lds0 + L_SYNC + SY_KVW);
  const __amdgpu_buffer_rsrc_t rsk = MKRS(WS_KV + (unsigned)(mt >> 1) * REC, 2 * REC);
  const int hh = g & 1, vo = REC + 1152 + (mt & 1) * 640 + hh * 320 + m * 16;
  if (m == 2) p[3] = u32x2{0x3f803f80u, 0x3f803f80u};
  auto r0 = __builtin_amdgcn_permlane32_swap(p[0][0], p[2][0], false, false), r1 = __builtin_amdgcn_permlane32_swap(p[0][1], p[2][1], false, false);
  auto r2 = __builtin_amdgcn_permlane32_swap(p[3][0], p[3][0], false, false), r3 = __builtin_amdgcn_permlane32_swap(p[3][1], p[3][1], false, false);
  __builtin_amdgcn_raw_buffer_store_b128(u32x4{r0[0], r1[0], r0[1], r1[1]}, rsk, g < 2 ? (16 * (mt & 1) + m + 32 * hh) * 16 : vo, 0, 16);
  if (g < 2 && m <= 2) __builtin_amdgcn_raw_buffer_store_b128(u32x4{r2[0], r3[0], r2[1], r3[1]}, rsk, vo + 256, 0, 16);
  if (g == 0) __builtin_amdgcn_raw_buffer_store_b32(p[1][0], rsk, 1024 + (mt & 1) * 1408 + m * 4, 0, 16);
  if (mt == 127) {
    __builtin_amdgcn_raw_buffer_store_b128(u32x4{0u, 0u, 0u, 0u}, rsk, REC + lane * 16, 0, 16);
    if (lane < 8) __builtin_amdgcn_raw_buffer_store_b128(u32x4{0u, 0u, 0u, 0u}, rsk, REC + 1024 + (lane >> 2) * 1408 + (lane & 3) * 16, 0, 16);
  }
  waitv<0>();
  gsignal(flags + eword(SL_KV, mt), lane == 0);
  lds_set(lds0 + L_SYNC + SY_KD);
}

constexpr int M_OFF = 90;
constexpr float L_TRIG = 9.5367431640625e-07f;
constexpr float L_MAX = 1.3292280e36f;
constexpr float L_MIN = 1e-29f;
constexpr float M_MAX = 4194304.f;

DI void set_negm(u32x2& q1, float m, int h) {
  const float nm = -m;
  const unsigned hi = __float_as_uint(nm) & 0xffff0000u;
  const float r1 = nm - __uint_as_float(hi);
  const unsigned mid = __float_as_uint(r1) & 0xffff0000u;
  const float r2 = r1 - __uint_as_float(mid);
  q1[1] = h ? (__float_as_uint(r2) >> 16) : ((hi >> 16) | mid);
}
DI float half_max(float v) {
  auto rr = __builtin_amdgcn_permlane32_swap(__float_as_uint(v), __float_as_uint(v), false, false);
  return __builtin_fmaxf(__uint_as_float(rr[0]), __uint_as_float(rr[1]));
}
DI float tile_max(const f32x16& s) {
  float a = __builtin_fmaxf(s[0], s[1]);
#pragma unroll
  for (int r = 2; r < 16; ++r) a = __builtin_fmaxf(a, s[r]);
  return half_max(a);
}
#define MFMA8(a, b) __builtin_amdgcn_mfma_f32_32x32x8bf16_1k(__builtin_bit_cast(s16x4, a), __builtin_bit_cast(s16x4, b), f32x16{}, 0, 0, 0)
DI f32x16 attn_slow(const char* kv, bf16x8 q0, u32x2 q1, int lane) {
  const int m = lane & 31, h = lane >> 5, vr = m < DH ? m : DH;
  q1[1] = 0u;
  __builtin_amdgcn_fence(__ATOMIC_ACQUIRE, "agent");
  float mx = -__builtin_inff();
  f32x16 o = f32x16{};
#pragma unroll 1
  for (int t = 0; t < 64; ++t) {
    const char* rk = kv + (size_t)t * REC; const char* rv = rk + REC + 1152 + h * 320 + vr * 16;
    const bf16x8 k0 = *(const bf16x8*)(rk + lane * 16);
    const u32x2 k1 = {*(const unsigned*)(rk + 1024 + m * 4 + (m >> 4) * 1344), 0u};
    const bf16x8 v0 = *(const bf16x8*)rv, v1 = *(const bf16x8*)(rv + 640);
    f32x16 s = MFMA8(k1, q1);
    s = MFMA(k0, q0, s);
    const float mn = __builtin_fmaxf(mx, tile_max(s));
    const float al = __builtin_amdgcn_exp2f(mx - mn);
    mx = mn;
    u32x4 p0, p1;
#pragma unroll
    for (int j = 0; j < 4; ++j) {
      p0[j] = cvtpk(__builtin_amdgcn_exp2f(s[2 * j] - mn), __builtin_amdgcn_exp2f(s[2 * j + 1] - mn));
      p1[j] = cvtpk(__builtin_amdgcn_exp2f(s[8 + 2 * j] - mn), __builtin_amdgcn_exp2f(s[8 + 2 * j + 1] - mn));
    }
#pragma unroll
    for (int r = 0; r < 16; ++r) o[r] *= al;
    o = MFMA(v0, BC(p0), o);
    o = MFMA(v1, BC(p1), o);
  }
  return o;
}
DI unsigned relu2pk(float a, float b) {
  unsigned r; float t;
  asm volatile("v_mul_f32_e64 %0, %2, |%2|\n\tv_mul_f32_e64 %1, %3, |%3|\n\tv_cvt_pk_bf16_f32 %0, %0, %1\n\tv_pk_max_i16 %0, %0, 0" : "=&v"(r), "=&v"(t) : "v"(a), "v"(b));
  return r;
}
DI void mfma_dc(f32x4& d, bf16x8 a, bf16x8 b, const f32x4& c) { asm volatile("v_mfma_f32_16x16x32_bf16 %0, %1, %2, %3" : "=&v"(d) : "v"(a), "v"(b), "v"(c)); }
DI void mfma_acc(f32x4& c, bf16x8 a, bf16x8 b) { asm volatile("v_mfma_f32_16x16x32_bf16 %0, %1, %2, %0" : "+v"(c) : "v"(a), "v"(b)); }
DI void mfma_acc16(f32x4& c, s16x4 a, u32x2 b) { asm volatile("v_mfma_f32_16x16x16_bf16 %0, %1, %2, %0" : "+v"(c) : "v"(a), "v"(b)); }
DI void mfma_z(f32x4& d, bf16x8 a, bf16x8 b) { asm volatile("v_mfma_f32_16x16x32_bf16 %0, %1, %2, 0" : "=&v"(d) : "v"(a), "v"(b)); }
template <int OFF>
DI void bload8_sc1(u32x2& dst, unsigned voff, u32x4 srd) { asm volatile("buffer_load_dwordx2 %0, %1, %2, 0 offen offset:%3 sc1" : "=v"(dst) : "v"(voff), "s"(srd), "i"(OFF) : "memory"); }

template <int N, class Fn> DI void static_for(Fn&& f) {
  if constexpr (N > 0) { static_for<N - 1>(f); f(std::integral_constant<int, N - 1>{}); }
}

extern "C" __global__ __launch_bounds__(512, 2) void k_main(const u16* __restrict__ x, const u16* __restrict__ wq, const u16* __restrict__ wk, const u16* __restrict__ wv, const u16* __restrict__ wo, const u16* __restrict__ w1,
                                                 const u16* __restrict__ b1, const u16* __restrict__ w2, const u16* __restrict__ b2, char* __restrict__ ws, u16* __restrict__ out) {
  extern __shared__ __attribute__((aligned(16))) char smem[];
  const int xcd = blockIdx.x & 7, bidx = blockIdx.x >> 3;
  const int kq = xcd & 3, nq = bidx & 3, cg = kq * 4 + nq, rb = (xcd >> 2) * 8 + (bidx >> 2);
  const int tid = threadIdx.x, lane = tid & 63, m = lane & 31, h = lane >> 5;
  const int w8 = __builtin_amdgcn_readfirstlane(tid >> 6), w = w8 & 3;
  const int mt = rb * 4 + w;
  const unsigned lds0 = (unsigned)(uintptr_t)(lds_ptr)smem;
  const unsigned voff = lane * 16;
  const int n0q = (36 * cg) & ~7;
  gu32* flags = (gu32*)(ws + WS_FLAGS);

  const bool lnblk = (nq & 1) == 0, leader = blockIdx.x == 8;
  gu32* beat = flags + (FL_BEAT + ((leader ? lane : blockIdx.x) & 31) * 256) / 4;
  unsigned b0 = 0u;
  if (w8 < 6) {
    if (lnblk) ln_kv(x, wk, wv, ws, (lds_ptr)smem, rb * 8 + kq * 2 + (nq >> 1), w8, lane);
    else {
      if (leader) {
        __builtin_amdgcn_raw_buffer_store_b128(u32x4{0u, 0u, 0u, 0u}, MKRS(WS_FLAGS + FL_E, 96 * 256), (unsigned)((w8 * 16 + (lane >> 2)) * 256 + (lane & 3) * 16), 0, 16);
      }
      asm volatile("s_barrier" ::: "memory");
      if (leader) { (void)lds_inc(lds0 + L_SYNC + SY_Z); lds_wait(lds0 + L_SYNC + SY_GO); __builtin_amdgcn_s_sleep(24); }
      const u32x4 s1 = mksrd(w1, D * F * 2);
      const int id = ((bidx >> 2) * 2 + (nq >> 1)) * 384 + w8 * 64 + lane;
      const int li = (xcd >> 2) * 5184 + id, row = li / 18, rq = id / 3;
      const u32x4 sq = mksrd(wq, D * D * 2);
      unsigned j0, j1;
      asm volatile("buffer_load_dword %1, %4, %5, 0 offen\n\tbuffer_load_dword %0, %2, %3, 0 offen\n\ts_waitcnt vmcnt(0)"
                   : "=&v"(j0), "=&v"(j1) : "v"(id < 5184 ? (unsigned)(row * F + 1152 * kq + (li - row * 18) * 64) * 2u : 0x7fffffffu), "s"(s1),
                     "v"(id < 1728 ? (unsigned)(rq * D + 144 * kq + (id - rq * 3) * 64 + ((id - rq * 3) >> 1) * 14) * 2u : 0x7fffffffu), "s"(sq) : "memory");
    }
  } else {
    b0 = __hip_atomic_load(beat, RLX_AGENT);
    if (lnblk && w8 == 7) __hip_atomic_store(flags + (rb * 8 + kq * 2 + (nq >> 1)) * 64 + lane, 0u, RLX_AGENT);
    const int t2 = tid & 127;
    if (t2 < 16) *(unsigned*)(smem + L_SYNC + t2 * 4) = 0u;
    *(unsigned*)(smem + L_CNT + t2 * 4) = 0u;
    asm volatile("s_waitcnt lgkmcnt(0)\n\ts_barrier" ::: "memory");
    if (w8 == 7) {
      if (leader) {
        lds_wait_ge(lds0 + L_SYNC + SY_Z, 6u);
        if (lane < 32) __hip_atomic_store(beat, ++b0, RLX_AGENT);
        gsignal(flags + eword(SL_ACK, blockIdx.x), lane == 0);
      } else {
        __builtin_amdgcn_s_sleep(20);
#pragma unroll 1
        for (unsigned i = 0; i < (1u << 18); ++i) {
          if (__builtin_amdgcn_readfirstlane(__hip_atomic_load(beat, RLX_AGENT)) != __builtin_amdgcn_readfirstlane(b0)) break;
          __builtin_amdgcn_s_sleep(8);
        }
        gsignal(flags + eword(SL_ACK, blockIdx.x), lane == 0 && !lnblk);
      }
      lds_set(lds0 + L_SYNC + SY_GO);
    }
  }
  if (w8 >= 4) {
    const int prow = lane >> 2, pch = (lane & 3) ^ ((lane >> 5) << 1);
    if (w8 == 6) {
      if (leader) lds_wait(lds0 + L_SYNC + SY_GO);
      else __builtin_amdgcn_s_sleep(12);
      unsigned bv[5];
#pragma unroll
      for (int i = 0; i < 5; ++i) bv[i] = b1[288 * cg + (lane + 64 * i < 288 ? lane + 64 * i : 0)];
      const u32x4 srdQ = mksrd(wq, D * D * 2);
      const unsigned vq0 = (unsigned)((prow * D + n0q + pch * 8) * 2), vq1 = (unsigned)((lane * D + n0q + 32) * 2);
#pragma unroll 1
      for (int q = 0; q < 9; ++q) {
        static_for<4>([&](auto i) { bdma16<L_G + i * 1024>(vq0, srdQ, (unsigned)((4 * q + i) * 16 * D * 2), lds0 + q * 4096); });
        bdma16<L_Q1>(vq1, srdQ, (unsigned)(q * 64 * D * 2), lds0 + q * 1024);
      }
      float* biasT = (float*)(smem + L_BIAS);
#pragma unroll
      for (int i = 0; i < 5; ++i) if (lane + 64 * i < 288) biasT[lane + 64 * i] = __uint_as_float(bv[i] << 16);
      (void)lds_inc(lds0 + L_SYNC + SY_ST);
    }
    if (lnblk) {
      if (w8 == 7) { kv_out(ws, (lds_ptr)smem, rb * 8 + kq * 2 + (nq >> 1), lane); gsignal(flags + eword(SL_ACK, blockIdx.x), lane == 0); }
      lds_wait(lds0 + L_SYNC + SY_KVW);
      lds_wait(lds0 + L_SYNC + SY_XN);
      __builtin_amdgcn_s_sleep(8);
    }
    const u32x4 srd1 = mksrd(w1, D * F * 2);
    const unsigned v1 = (unsigned)((prow * F + pch * 8 + 288 * cg) * 2);
    static_for<9>([&](auto p) { bdma16<L_G + GSLOT + p * 1024>(v1, srd1, (unsigned)((9 * w + p) * 16 * F * 2), lds0 + 9 * w * 1024); });
    (void)lds_inc(lds0 + L_SYNC + SY_ST2);
    static_for<9>([&](auto p) { bdma16<L_G + 2 * GSLOT + p * 1024>(v1 + 64u, srd1, (unsigned)((9 * w + p) * 16 * F * 2), lds0 + 9 * w * 1024); });
    if (w8 != 4) { waitv<0>(); arrive<C_L>(lds0 + L_CNT + lane * 4, 2u); }
  }
  if (leader && w8 == 7) {
#pragma unroll 1
    for (unsigned i = 0; i < (1u << 18); ++i) {
      if (lane < 32) __hip_atomic_store(beat, ++b0, RLX_AGENT);
      unsigned ok = 1u;
#pragma unroll
      for (int k = 0; k < 4; ++k) ok &= __hip_atomic_load(flags + eword(SL_ACK, k * 64 + lane), RLX_AGENT);
      if (__all(ok != 0u)) break;
      __builtin_amdgcn_s_sleep(24);
    }
  }
  if (w8 == 4) {
    lds_wait(lds0 + L_SYNC + SY_XN);
#pragma unroll 1
    for (unsigned i = 0; i < (1u << 18); ++i) {
      const unsigned f0 = __hip_atomic_load(flags + eword(SL_KV, lane), RLX_AGENT), f1 = __hip_atomic_load(flags + eword(SL_KV, 64 + lane), RLX_AGENT);
      if (__all((f0 & f1) != 0u)) break;
      __builtin_amdgcn_s_sleep(16);
    }
    lds_set(lds0 + L_SYNC + SY_KV);
    waitv<0>(); arrive<C_L>(lds0 + L_CNT + lane * 4, 2u);
  }

  if (w8 < 4) {
    const int prow = lane >> 2, pch = (lane & 3) ^ ((lane >> 5) << 1);
    const int g4 = lane >> 4, i16 = lane & 15;
    const unsigned ldsw = lds0 + L_G + 9 * w * 1024;
    bf16x8 xf[36];
    const char* xb = ws + WS_XNF + (size_t)mt * 36 * 1024;
    auto xload = [&](auto uc) { constexpr int u = uc, o = ((u & 1) * 18 + (u >> 1)) * 1024; gload16<(o & 4095)>(xf[u], voff, xb + (o & ~4095)); };
    lds_wait(lds0 + L_SYNC + SY_GO);
    if (!lnblk) __builtin_amdgcn_s_sleep(16);
    (void)gpoll<6>(flags + eword(SL_XN, mt * 16 + i16), 0xfffu);
    if (lnblk) lds_wait(lds0 + L_SYNC + SY_KD);
    if (w8 == 0) lds_set(lds0 + L_SYNC + SY_XN);
    static_for<36>([&](auto u) { xload(u); });
    u32x4 srdW = mksrd(w1, D * F * 2);
    unsigned so[9];
#pragma unroll
    for (int p = 0; p < 9; ++p) so[p] = (unsigned)((9 * w + p) * 16 * F * 2 - (p & 3) * 1024);
    unsigned voffA = (unsigned)((prow * F + pch * 8 + 288 * cg) * 2) + 64u, voffO = voffA, voffB = voffA;
    PIN(voffO); PIN(voffB);
    unsigned aC = lds0 + L_CNT + lane * 4, one = 1u, tgt = 4u;
    PIN(aC); PIN(one);
    lds_ptr pC = (lds_ptr)smem + L_CNT + lane * 4; PIN(pC);
    const int rdo = (8 * g4 + (i16 >> 2)) * 64 + (g4 & 1) * 32 + (i16 & 3) * 8;
    const lds_ptr rd0 = (lds_ptr)smem + L_G + rdo, rd1 = (lds_ptr)smem + L_G + (rdo ^ 32);
    lds_ptr biasp = (lds_ptr)smem + L_BIAS + g4 * 16;
    lds_wait(lds0 + L_SYNC + SY_ST);
    __builtin_amdgcn_s_setprio(2);
    s16x4 wr[18];
    f32x4 A[4], P[4], Bi[2];
    {
      const lds_ptr rq1 = (lds_ptr)smem + L_Q1 + (8 * g4 + (i16 >> 2)) * 16 + (i16 & 1) * 8;
      auto qread = [&](auto itc) {
        constexpr int it = itc, st = it / 6, tl = (it % 6) >> 1, hf = it & 1;
        if constexpr (st < 18) {
          if constexpr (tl == 0) wr[it % 12] = trread(rd0 + st * 2048 + hf * 256);
          else if constexpr (tl == 1) wr[it % 12] = trread(rd1 + st * 2048 + hf * 256);
          else wr[it % 12] = trread(rq1 + st * 512 + hf * 64);
        }
      };
      static_for<11>([&](auto it) { qread(it); });
      static_for<18>([&](auto ksc) {
        constexpr int ks = ksc;
        waitv<34 - 2 * ks>();
        static_for<6>([&](auto sc) {
          constexpr int sI = sc, tl = sI >> 1, mi = sI & 1, gi = 6 * ks + sI;
          const bf16x8 wf = cat8(wr[(6 * ks + 2 * tl) % 12], wr[(6 * ks + 2 * tl + 1) % 12]);
          f32x4& Dd = tl < 2 ? A[sI & 3] : P[mi];
          if constexpr (ks == 0) mfma_z(Dd, wf, xf[2 * ks + mi]); else mfma_acc(Dd, wf, xf[2 * ks + mi]);
          SBAR();
          qread(std::integral_constant<int, gi + 11>{});
          if constexpr (gi + 11 == 107) arrive<C_R>(aC, one);
          SBAR();
        });
      });
      asm volatile("s_nop 7" : "+v"(A[0]), "+v"(A[1]), "+v"(A[2]), "+v"(A[3]), "+v"(P[0]), "+v"(P[1]));
      const lds_ptr qw = (lds_ptr)smem + L_Q + (32 * w + i16) * QROW + g4 * 8;
#pragma unroll
      for (int i = 0; i < 4; ++i) *(LAS u32x2*)(qw + (i & 1) * 16 * QROW + (i >> 1) * 32) = u32x2{cvtpk(A[i][0] * C2, A[i][1] * C2), cvtpk(A[i][2] * C2, A[i][3] * C2)};
      if (g4 < 2) {
#pragma unroll
        for (int i = 0; i < 2; ++i) *(LAS u32x2*)(qw + i * 16 * QROW + 64) = u32x2{cvtpk(P[i][0] * C2, P[i][1] * C2), cvtpk(P[i][2] * C2, P[i][3] * C2)};
      }
      lds_set(lds0 + L_SYNC + SY_Q + w * 4);
      P[2] = P[0]; P[3] = P[1];
      lds_wait_ge(lds0 + L_SYNC + SY_ST2, 4u);
    }
    const unsigned vA2 = (unsigned)((((lane >> 2) & 3) * D + (lane >> 4) * 32 + (lane & 3) * 8 + 144 * nq) * 2);
    const unsigned vO2 = (unsigned)((((lane >> 2) & 3) * D + (lane >> 4) * 32 + ((lane & 3) ^ 2) * 8 + 144 * nq) * 2);
    unsigned vB2 = (unsigned)(((lane >> 1) * D + 128 + (lane & 1) * 8 + 144 * nq) * 2);
    auto kpos = [&](int i) { int u = (i < 2 ? i + 7 : (i < 9 ? i - 2 : i)) + 9 * nq; return u >= 36 ? u - 36 : u; };
    const unsigned tabD = lane < 9 ? (unsigned)((1152 * kq + 32 * kpos(4 * lane + w)) * D * 2) : (unsigned)((144 * kq + 36 * w) * D * 2);
    const unsigned tabH = lane < 36 ? (unsigned)kpos(lane) * 2048u : (unsigned)(lane & 3) * 2048u;
    auto set_g2 = [&](int j) {
      const unsigned r = __builtin_amdgcn_readlane(tabD, j);
      voffA = vA2 + r; voffO = vO2 + r; voffB = vB2 + r;
    };
    auto bias_read = [&]() { Bi[0] = *(const LAS f32x4*)biasp; Bi[1] = *(const LAS f32x4*)(biasp + 64); biasp += 128; };
    const __amdgpu_buffer_rsrc_t rsh = MKRS(WS_HF + (unsigned)(mt * 288 + 18 * cg) * 1024, 18 * 1024);
    unsigned hst = voff + 16384u;
    unsigned ep[8] = {}, linc = 0u;
    int sl_cur = GSLOT, sl_dma = 0;
    lds_ptr ra = rd0 + GSLOT, rb1 = rd1 + GSLOT;
    auto wread1 = [&](auto itc) { constexpr int it = itc, st = (it >> 2) % 18; wr[it % 12] = trread(((it & 2) ? rb1 : ra) + st * 2048 + (it & 1) * 256); };
    auto dma_piece = [&](auto pc) { constexpr int p = pc; bdma_m0<(p & 3) * 1024>(p == 8 ? voffB : ((p & 1) ? voffO : voffA), srdW, so[p]); };
    auto pin_so = [&]() {
#pragma unroll
      for (int p = 0; p < 9; ++p) PINS(so[p]);
    };
    bias_read();
    static_for<11>([&](auto it) { wread1(it); });
#pragma unroll 1
    for (int t = 0; t < 9; ++t) {
      voffA += 64; voffO += 64; voffB += 64;
      if (__builtin_expect(t >= 7, 0)) {
        srdW = mksrd(w2, F * D * 2);
#pragma unroll
        for (int p = 0; p < 9; ++p) so[p] = p < 8 ? (unsigned)(p * 4 * D * 2 - (p & 3) * 1024) : 0u;
        set_g2(t - 7);
        __builtin_amdgcn_s_setprio(0);
      }
      pin_so();
      const unsigned ldsd = ldsw + sl_dma;
      const int sl_nxt = sl_cur == 2 * GSLOT ? 0 : sl_cur + GSLOT;
      unsigned cv;
      static_for<72>([&](auto gc) {
        constexpr int gi = gc, ks = gi >> 2, tl = (gi >> 1) & 1, mi = gi & 1;
        if constexpr ((gi & 3) == 0) asm volatile("" :: "v"(wr[gi % 12 + 3]));
        const bf16x8 wf = cat8(wr[(4 * ks + 2 * tl) % 12], wr[(4 * ks + 2 * tl + 1) % 12]);
        if constexpr (ks == 0) mfma_dc(A[gi & 3], wf, xf[2 * ks + mi], Bi[tl]);
        else if constexpr (ks == 17) mfma_dc(P[gi & 3], wf, xf[2 * ks + mi], A[gi & 3]);
        else mfma_acc(A[gi & 3], wf, xf[2 * ks + mi]);
        SBAR();
        if constexpr (gi == 11) { check<C_R>(cv, aC, tgt); tgt += 4u; }
        if constexpr (gi == 61) { check<C_L>(cv, aC, tgt); ra = rd0 + sl_nxt; rb1 = rd1 + sl_nxt; }
        if constexpr (gi == 12 || gi == 40 || gi == 68) set_m0<(gi - 12) / 28 * 4096>(ldsd);
        wread1(std::integral_constant<int, gi + 11>{});
        if constexpr (gi == 0) cv = *(volatile LAS unsigned*)(pC + C_R);
        if constexpr (gi == 52) cv = *(volatile LAS unsigned*)(pC + C_L);
        if constexpr (gi == 60) arrive<C_R>(aC, one);
        if constexpr (gi >= 12 && (gi - 12) % 7 == 0) dma_piece(std::integral_constant<int, (gi - 12) / 7>{});
        if constexpr (gi >= 6 && gi <= 34 && gi % 4 == 2) {
          constexpr int j = (gi - 6) / 4, pi = ((j >> 1) & 1) * 2 + (j >> 2);
          ep[j] = relu2pk(P[pi][2 * (j & 1)], P[pi][2 * (j & 1) + 1]);
        }
        if constexpr (gi == 23 || gi == 37) {
          constexpr int hh = gi == 37;
          __builtin_amdgcn_raw_buffer_store_b128(u32x4{ep[4 * hh], ep[4 * hh + 1], ep[4 * hh + 2], ep[4 * hh + 3]}, rsh, hst + hh * 1024, 0, 16);
        }
        if constexpr (gi == 46) { waitv<7>(); arrive<C_L>(aC, linc); linc = 1u; PIN(linc); hst = voff + (unsigned)t * 2048u; PIN(hst); }
        if constexpr (gi == 44) bias_read();
        SBAR();
      });
      sl_dma = sl_cur; sl_cur = sl_nxt;
    }
    u32x4 hf[8];
    u32x4 srdH = mksrd(ws + WS_HF + ((size_t)mt * 288 + 72 * kq) * 1024, 72 * 1024);
    const u32x4 srdX = mksrd(ws + WS_OX + (size_t)(mt * 16 + 4 * kq) * 256, 1024);
    unsigned sh[4];
    auto set_h = [&](int j) {
#pragma unroll
      for (int i = 0; i < 4; ++i) sh[i] = __builtin_amdgcn_readlane(tabH, 4 * j + i);
    };
    set_h(0);
    asm volatile("s_nop 4" : "+s"(sh[2]), "+s"(sh[3]));
    static_for<4>([&](auto i) { bload16_sc1<(i & 1) * 1024>(hf[4 + i], voff, srdH, sh[2 + (i >> 1)]); });
    hf[0] = u32x4{ep[0], ep[1], ep[2], ep[3]}; hf[1] = u32x4{ep[4], ep[5], ep[6], ep[7]};
    const int r2o = g4 * 1024 + (i16 >> 2) * 64 + (g4 & 1) * 32 + (i16 & 3) * 8;
    const lds_ptr r2a0 = (lds_ptr)smem + L_G + r2o, r2b0 = (lds_ptr)smem + L_G + (r2o ^ 32);
    const lds_ptr r2h0 = (lds_ptr)smem + L_G + 8192 + (4 * g4 + (i16 >> 2)) * 32 + (i16 & 3) * 8;
    lds_ptr r2a = r2a0 + sl_cur, r2b = r2b0 + sl_cur, r2h = r2h0 + sl_cur;
    auto wread2 = [&](auto itc) {
      constexpr int it = itc, ks = (it / 18) & 3, nt = (it % 18) >> 1, hi = it & 1;
      if constexpr (nt < 8) wr[it % 18] = trread(((nt & 1) ? r2b : r2a) + ks * 9216 + hi * 4096 + (nt >> 1) * 256);
      else wr[it % 18] = trread(r2h + ks * 9216 + hi * 512);
    };
    static_for<17>([&](auto it) { wread2(it); });
    asm volatile("s_nop 7" : "+v"(P[0]), "+v"(P[1]), "+v"(P[2]), "+v"(P[3]));
#pragma unroll
    for (int j = 0; j < 8; ++j) { const int pi = ((j >> 1) & 1) * 2 + (j >> 2); ep[j] = relu2pk(P[pi][2 * (j & 1)], P[pi][2 * (j & 1) + 1]); }
    hf[2] = u32x4{ep[0], ep[1], ep[2], ep[3]}; hf[3] = u32x4{ep[4], ep[5], ep[6], ep[7]};
    __builtin_amdgcn_raw_buffer_store_b128(hf[2], rsh, hst, 0, 16);
    __builtin_amdgcn_raw_buffer_store_b128(hf[3], rsh, hst + 1024, 0, 16);
    f32x4 acc[18] = {};
    const unsigned voffX = (unsigned)(g4 * 256 + i16 * 8);
    lds_wait(lds0 + L_SYNC + SY_KV);
    gu32* fline = flags + (mt * 4 + kq) * 32;
    const unsigned foff = (unsigned)i16 * 4u;
    unsigned fl = 0u;
    const unsigned tabN = lane == 8 ? (0xf0u | (0x100u << nq)) : (((lane & 1) && lane < 6) ? 1u << ((nq + ((lane + 1) >> 1)) & 3) : 0u);
    u32x2 ox0, ox1;
#pragma unroll 1
    for (int j = 0; j < 9; ++j) {
      const int sl_nxt = sl_cur == 2 * GSLOT ? 0 : sl_cur + GSLOT;
      unsigned need = __builtin_amdgcn_readlane(tabN, j);
      if (__builtin_expect(j >= 7, 0)) {
        if (j == 7) {
          srdW = mksrd(wo, D * D * 2);
#pragma unroll
          for (int p = 4; p < 8; ++p) so[p] += 2 * D * 2;
          vB2 += (unsigned)(h * 2 * D * 2);
        } else {
          srdW[2] = 0u;
          srdH = mksrd(ws + WS_OF + ((size_t)mt * 32 + 8 * kq) * 1024, 8 * 1024);
        }
      }
      pin_so();
      set_g2(j + 2); set_h(j + 1);
      const unsigned ldsd = ldsw + sl_dma;
      unsigned cv;
      static_for<72>([&](auto gc) {
        constexpr int gi = gc, ks = gi / 18, nt = (gi % 18) >> 1, mi = gi & 1;
        if constexpr (gi % 18 == 0) { waitv<(ks == 0 ? 14 : (ks == 1 ? 13 : (ks == 2 ? 11 : 14)))>(); if constexpr (ks == 2) arrive<C_L>(aC, one); }
        if constexpr (gi % 6 == 0) asm volatile("" :: "v"(wr[gi % 18 + 5]));
        const bf16x8 wf = cat8(wr[2 * nt], wr[2 * nt + 1]);
        mfma_acc(acc[gi % 18], wf, BC(hf[2 * ks + mi]));
        SBAR();
        if constexpr (gi == 5) { check<C_R>(cv, aC, tgt); tgt += 4u; }
        if constexpr (gi == 55) { check<C_L>(cv, aC, tgt); r2a = r2a0 + sl_nxt; r2b = r2b0 + sl_nxt; r2h = r2h0 + sl_nxt; }
        constexpr int dp = gi == 6 ? 0 : gi == 11 ? 1 : gi == 21 ? 2 : gi == 26 ? 3 : gi == 31 ? 4 : gi == 40 ? 5 : gi == 46 ? 6 : gi == 58 ? 7 : gi == 64 ? 8 : -1;
        if constexpr (dp >= 0 && (dp & 3) == 0) set_m0<dp * 1024>(ldsd);
        wread2(std::integral_constant<int, gi + 17>{});
        if constexpr (gi == 0) cv = *(volatile LAS unsigned*)(pC + C_R);
        if constexpr (gi == 44) cv = *(volatile LAS unsigned*)(pC + C_L);
        if constexpr (gi == 54) arrive<C_R>(aC, one);
        if constexpr (dp >= 0) dma_piece(std::integral_constant<int, (dp >= 0 ? dp : 0)>{});
        if constexpr (gi == 15) {
          if (need) {
            waitv<8>(); PIN(fl);
            if (((unsigned)__ballot(fl != 0u) & need) != need) { (void)gpoll<1>(fline + i16, need); }
          }
        }
        if constexpr (gi == 49) asm volatile("global_load_dword %0, %1, %2 sc1" : "+v"(fl) : "v"(foff), "s"(fline) : "memory");
        if constexpr (gi % 18 >= 16) bload16_sc1<mi * 1024>(hf[2 * ks + mi], voff, srdH, sh[ks]);
        if constexpr (gi == 24) { if (j == 0) { waitv<5>(); gsignal(fline + nq, lane == 0); } }
        SBAR();
      });
      sl_dma = sl_cur; sl_cur = sl_nxt;
    }
    bload8_sc1<0>(ox0, voffX, srdX);
    bload8_sc1<128>(ox1, voffX, srdX);
    s16x4 wx[9];
    static_for<54>([&](auto gc) {
      constexpr int gi = gc, ks = gi / 18, nt = (gi % 18) >> 1, mi = gi & 1;
      if constexpr (gi % 18 == 0) waitv<(ks == 0 ? 16 : (ks == 1 ? 11 : 6))>();
      if constexpr (gi % 6 == 0) asm volatile("" :: "v"(wr[gi % 18 + 5]));
      const bf16x8 wf = cat8(wr[2 * nt], wr[2 * nt + 1]);
      mfma_acc(acc[gi % 18], wf, BC(hf[2 * ks + mi]));
      SBAR();
      wread2(std::integral_constant<int, gi + 17>{});
      if constexpr (gi >= 20 && gi < 47 && gi % 3 == 2) {
        constexpr int e = (gi - 20) / 3;
        wx[e] = e < 8 ? trread(((e & 1) ? r2b0 : r2a0) + (L_EX - L_G + (e >> 1) * 256)) : trread(r2h0 + (L_EX + 4096 - L_G - 8192));
      }
      SBAR();
    });
    wread2(std::integral_constant<int, 71>{});
    waitv<0>();
    asm volatile("" : "+v"(hf[6]), "+v"(hf[7]) : "v"(fl));
    static_for<18>([&](auto ic) { constexpr int i = ic; mfma_acc16(acc[i], wx[i >> 1], (i & 1) ? ox1 : ox0); });
    SBAR();
    const __amdgpu_buffer_rsrc_t rsp = MKRS(WS_PART + (unsigned)((mt * 4 + nq) * 4 + kq) * 9 * 1024, 9 * 1024);
    auto finish = [&](auto own) {
      static_for<11>([&](auto nc) {
        constexpr int nt = nc, d = nt - 2;
        if constexpr (nt < 9) {
          const bf16x8 wf = cat8(wr[2 * nt], wr[2 * nt + 1]);
          acc[2 * nt] = M16(wf, BC(hf[6]), acc[2 * nt]);
          acc[2 * nt + 1] = M16(wf, BC(hf[7]), acc[2 * nt + 1]);
          SBAR();
        }
        if constexpr (d >= 0) {
          if constexpr (own) {
            *(LAS f32x4*)((lds_ptr)smem + L_KV + d * 2048 + voff) = acc[2 * d];
            *(LAS f32x4*)((lds_ptr)smem + L_KV + d * 2048 + 1024 + voff) = acc[2 * d + 1];
          } else {
            auto r0 = __builtin_amdgcn_permlane16_swap(cvtpk(acc[2 * d][0], acc[2 * d][1]), cvtpk(acc[2 * d + 1][0], acc[2 * d + 1][1]), false, false);
            auto r1 = __builtin_amdgcn_permlane16_swap(cvtpk(acc[2 * d][2], acc[2 * d][3]), cvtpk(acc[2 * d + 1][2], acc[2 * d + 1][3]), false, false);
            __builtin_amdgcn_raw_buffer_store_b128(u32x4{r0[0], r1[0], r0[1], r1[1]}, rsp, voff + d * 1024, 0, 16);
          }
        }
        SBAR();
      });
    };
    if (w != kq) finish(std::false_type{});
    else { finish(std::true_type{}); lds_set(lds0 + L_SYNC + SY_F); }
  } else {
    __builtin_amdgcn_s_setprio(1);
    const char* kv = ws + WS_KV;
    const unsigned ldsk = lds0 + L_KV + 5 * w * 1024;
    const char* kvsrc = kv + REC + 5 * w * 1024;
    lds_wait(lds0 + L_SYNC + SY_KV);
    bf16x8 kA0, kB0;
    unsigned k1t;
    u32x2 kA1 = {0u, 0x3f803f80u}, kB1 = {0u, 0x3f803f80u};
    asm volatile("global_load_dwordx4 %0, %2, %4 sc1\n\tglobal_load_dword %1, %3, %4 offset:1024 sc1" : "=&v"(kA0), "=&v"(k1t) : "v"(voff), "v"((unsigned)(m * 4 + (m >> 4) * 1344)), "s"(kv) : "memory");
    static_for<4>([&](auto p) { glds16<p * 1024, p * 1024>(voff, kvsrc, ldsk); });
    glds16<4096, 0>(voff, kvsrc + 4096, ldsk);
    bf16x8 vA0 = {}, vA1 = {}, vB0, vB1;
    f32x16 oa = {}, ob = {};
    u32x4 pa0, pa1, pb0 = {}, pb1 = {};
    const int vr = m < DH ? m : DH;
    lds_ptr kp0 = (lds_ptr)smem + L_KV + lane * 16, kp1 = (lds_ptr)smem + L_KV + 1024 + m * 4 + (m >> 4) * 1344, vp = (lds_ptr)smem + L_KV + 1152 + h * 320 + vr * 16;
    unsigned abar_addr = lds0 + L_SYNC + SY_A;
    const LAS unsigned* qrow = (const LAS unsigned*)((lds_ptr)smem + L_Q + (32 * w + m) * QROW + (36 * cg - n0q) * 2 + h * 8);
    PIN(vA0); PIN(vA1); PIN(oa); PIN(ob); PIN(pb0); PIN(pb1); PIN(kp0); PIN(kp1); PIN(vp); PIN(abar_addr); PIN(qrow); PIN(kB1);
    asm volatile("s_waitcnt vmcnt(5)" : "+v"(kA0), "+v"(k1t) :: "memory");
    kA1[0] = k1t; PIN(kA1);
    lds_wait(lds0 + L_SYNC + SY_Q + w * 4);
    u32x4 qa0 = {qrow[0], qrow[1], qrow[4], qrow[5]};
    u32x4 qb0 = {qrow[9], qrow[10], qrow[13], qrow[14]};
    u32x2 qa1 = {qrow[8], 0u}, qb1 = {qrow[17], 0u};
    qa1[0] = h ? 0u : qa1[0]; qb1[0] = h ? 0u : qb1[0];
    f32x16 sa = MFMA8(kA1, qa1); sa = MFMA(kA0, BC(qa0), sa);
    f32x16 sb = MFMA8(kA1, qb1); sb = MFMA(kA0, BC(qb0), sb);
    float ma = __builtin_rintf(tile_max(sa)) + (float)M_OFF, mb = __builtin_rintf(tile_max(sb)) + (float)M_OFF;
    const bool bad0 = !(ma > -M_MAX) || !(mb > -M_MAX);
    set_negm(qa1, ma, h); set_negm(qb1, mb, h);
#pragma unroll
    for (int r = 0; r < 16; ++r) sa[r] -= ma;
    unsigned pfj = 0u;
    const __amdgpu_buffer_rsrc_t rsp = MKRS(WS_PART + (unsigned)(((rb * 4 + kq) * 4 + nq) * 4) * 9 * 1024, 4 * 9 * 1024);
#define EX(v) __builtin_amdgcn_exp2f(v)
#define GAP(MF, SS, PW, i) do { MF; SBAR(); { const float e0 = EX(SS[4 * (i)]), e1 = EX(SS[4 * (i) + 1]), e2 = EX(SS[4 * (i) + 2]), e3 = EX(SS[4 * (i) + 3]); \
      PW[(2 * (i)) & 3] = cvtpk(e0, e1); PW[(2 * (i) + 1) & 3] = cvtpk(e2, e3); } PIN(PW); SBAR(); } while (0)
#define RESC(FL, O, SS, M, Q1) do { if (__builtin_expect(FL, 0)) { \
      int e = __builtin_amdgcn_frexp_expf(O[10]) + M_OFF; e = e > 0 ? e : 0; const float ef = (float)e; \
      _Pragma("unroll") for (int r = 0; r < 16; ++r) { O[r] = __builtin_amdgcn_ldexpf(O[r], -e); SS[r] -= ef; } \
      M += ef; set_negm(Q1, M, h); } SBAR(); } while (0)
#define ITER(tt, CK, KC0, KC1, VC0, VC1, KN0, KN1, VN0, VN1) do { \
    bool flb = false, fla = false; \
    GAP(sb = MFMA8(KC1, qb1); SBAR(); KN1[0] = *(const LAS unsigned*)(kp1 + (tt) * REC); KN0 = *(const LAS bf16x8*)(kp0 + (tt) * REC); if (CK) flb = __any(!(ob[10] <= L_TRIG)), sa, pa0, 0); \
    GAP(sb = MFMA(KC0, BC(qb0), sb); SBAR(); VN0 = *(const LAS bf16x8*)(vp + (tt) * REC); VN1 = *(const LAS bf16x8*)(vp + (tt) * REC + 640), sa, pa0, 1); \
    GAP(ob = MFMA(VC0, BC(pb0), ob), sa, pa1, 2); \
    GAP(ob = MFMA(VC1, BC(pb1), ob), sa, pa1, 3); \
    if (CK) RESC(flb, ob, sb, mb, qb1); \
    asm volatile("" :: "v"(VN1)); \
    GAP(sa = MFMA8(KN1, qa1); SBAR(); if (CK) fla = __any(!(oa[10] <= L_TRIG)), sb, pb0, 0); \
    GAP(sa = MFMA(KN0, BC(qa0), sa), sb, pb0, 1); \
    GAP(oa = MFMA(VN0, BC(pa0), oa), sb, pb1, 2); \
    GAP(oa = MFMA(VN1, BC(pa1), oa), sb, pb1, 3); \
    if (CK) RESC(fla, oa, sa, ma, qa1); \
  } while (0)
#pragma unroll 1
    for (int c = 0; c < 8; ++c) {
      {
        if (c >= 4 && c <= 6 && w != kq) swbar<3>(abar_addr, 4 * (c + 1)); else swbar<0>(abar_addr, 4 * (c + 1));
        if (c < 7) {
          const char* src = kvsrc + (size_t)(c + 1) * CHUNK; const unsigned dst = ldsk + ((c + 1) & 1) * CHUNK;
          static_for<4>([&](auto p) { glds16<p * 1024, p * 1024>(voff, src, dst); });
          glds16<4096, 0>(voff, src + 4096, dst);
        }
        if (c < 3) {
          const u32x4 s2 = mksrd(w2 + (size_t)1152 * kq * D, 1152 * D * 2);
          const unsigned li = lane < 27 ? (unsigned)(((c * 32 + bidx) * 4 + w) * 27 + lane) * 128u : 0x7fffffffu;
          asm volatile("buffer_load_dword %0, %1, %2, 0 offen" : "+v"(pfj) : "v"(li), "s"(s2) : "memory");
        }
        if (c == 2) {
          const u32x4 s2 = mksrd(wo + 144 * kq * D, 144 * D * 2);
          const unsigned li = lane < 11 ? (unsigned)((bidx * 4 + w) * 11 + lane) * 128u : 0x7fffffffu;
          asm volatile("buffer_load_dword %0, %1, %2, 0 offen" : "+v"(pfj) : "v"(li), "s"(s2) : "memory");
        }
        if (c >= 3 && c <= 5) {
          if (w != kq) {
#pragma unroll
            for (int i = 0; i < 3; ++i) __builtin_amdgcn_raw_buffer_store_b128(u32x4{~0u, ~0u, ~0u, ~0u}, rsp, voff + (unsigned)(w * 9216 + (c - 3) * 3072) + i * 1024, 0, 16);
          }
        } else if (c == 6) {
          const int r = (lane >> 2) & 3;
          const int grow = 36 * (4 * kq + w) + 16 + (r >> 1) * 18 + (r & 1);
          glds16<L_EX, 0>((unsigned)((grow * D + 144 * nq + (lane >> 4) * 32 + ((lane & 3) ^ ((w & 1) << 1)) * 8) * 2), wo, lds0 + w * 1024);
          const int rho = (lane >> 1) & 15;
          const int grow2 = 36 * (4 * kq + (rho >> 2)) + 16 + ((rho >> 1) & 1) * 18 + (rho & 1);
          glds16<L_EX + 4096, 0>((unsigned)((grow2 * D + 144 * nq + 128 + (lane & 1) * 8) * 2), wo, lds0);
        } else if (c == 7) gsignal(flags + ((rb * 4 + kq) * 4 + (lane & 3)) * 32 + 8 + nq, w == 0 && lane < 4);
      }
#pragma unroll
      for (int t2 = 0; t2 < 4; ++t2) {
        ITER(2 * t2, false, kA0, kA1, vA0, vA1, kB0, kB1, vB0, vB1);
        ITER(2 * t2 + 1, true, kB0, kB1, vB0, vB1, kA0, kA1, vA0, vA1);
      }
      PIN(pfj);
      const int dlt = (c & 1) ? -CHUNK : CHUNK;
      kp0 += dlt; kp1 += dlt; vp += dlt;
    }
    ob = MFMA(vA0, BC(pb0), ob); ob = MFMA(vA1, BC(pb1), ob);
#undef ITER
#undef RESC
#undef GAP
#undef EX
    {
      float ca = 0.f, cb = 0.f;
#pragma unroll
      for (int r = 0; r < 11; ++r) { ca = __builtin_fmaf(oa[r], 0.f, ca); cb = __builtin_fmaf(ob[r], 0.f, cb); }
      const bool bad = bad0 || !(ca == 0.f) || !(cb == 0.f) || !(oa[10] > L_MIN) || !(ob[10] > L_MIN) || !(oa[10] < L_MAX) || !(ob[10] < L_MAX) || !(ma < M_MAX) || !(mb < M_MAX);
      if (__builtin_expect(__any(bad), 0)) { oa = attn_slow(kv, BC(qa0), qa1, lane); ob = attn_slow(kv, BC(qb0), qb1, lane); }
    }
    {
      const float ra = 1.0f / oa[10], rbb = 1.0f / ob[10];
      const u32x4 f0 = {cvtpk(oa[0] * ra, oa[1] * ra), cvtpk(oa[2] * ra, oa[3] * ra), cvtpk(ob[0] * rbb, ob[1] * rbb), cvtpk(ob[2] * rbb, ob[3] * rbb)};
      const u32x4 f1 = {cvtpk(oa[4] * ra, oa[5] * ra), cvtpk(oa[6] * ra, oa[7] * ra), cvtpk(ob[4] * rbb, ob[5] * rbb), cvtpk(ob[6] * rbb, ob[7] * rbb)};
      const __amdgpu_buffer_rsrc_t rso = MKRS(WS_OF + (unsigned)(mt * 32 + 2 * cg) * 1024, 2048), rsx = MKRS(WS_OX + (unsigned)(mt * 16 + cg) * 256, 256);
      const unsigned oo = (unsigned)((m >> 4) * 1024 + ((m & 15) + 16 * h) * 16);
      __builtin_amdgcn_raw_buffer_store_b128(f0, rso, oo, 0, 16);
      __builtin_amdgcn_raw_buffer_store_b128(f1, rso, oo + 512, 0, 16);
      const u32x2 ex = {cvtpk(oa[8] * ra, oa[9] * ra), cvtpk(ob[8] * rbb, ob[9] * rbb)};
      if (h == 0) __builtin_amdgcn_raw_buffer_store_b64(ex, rsx, m * 8, 0, 16);
      waitv<0>();
      gsignal(flags + (mt * 4 + kq) * 32 + 4 + nq, lane == 0);
    }
    __builtin_amdgcn_s_setprio(0);
    {
      const int fm = rb * 4 + kq;
      const int p0 = w == 3 ? 6 : 2 * w, np = w == 3 ? 3 : 2;
      const int g4 = lane >> 4;
      const size_t rowoff = (size_t)(32 * fm + 16 * (g4 & 1) + (lane & 15)) * D + 144 * nq;
      u32x4 xv[3], bv[3];
#pragma unroll
      for (int i = 0; i < 3; ++i) { const int pi = i < np ? p0 + i : p0; xv[i] = *(const u32x4*)(x + rowoff + 16 * pi + 8 * h); bv[i] = *(const u32x4*)(b2 + 144 * nq + 16 * pi + 8 * h); }
      lds_wait(lds0 + L_SYNC + SY_F);
      u32x4 t[3][3];
      auto pload = [&]() {
#pragma unroll
        for (int q = 0; q < 3; ++q) {
          const int src = q < kq ? q : q + 1;
#pragma unroll
          for (int i = 0; i < 3; ++i) { const int pi = i < np ? p0 + i : p0; t[q][i] = __builtin_amdgcn_raw_buffer_load_b128(rsp, voff, (src * 9 + pi) * 1024, 16); }
        }
      };
      pload();
      SBAR();
      float base[3][8];
#pragma unroll
      for (int i = 0; i < 3; ++i) {
        const int pi = i < np ? p0 + i : p0;
        const f32x4 o0 = *(const LAS f32x4*)((lds_ptr)smem + L_KV + pi * 2048 + voff), o1 = *(const LAS f32x4*)((lds_ptr)smem + L_KV + pi * 2048 + 1024 + voff);
#pragma unroll
        for (int e = 0; e < 4; ++e) {
          auto r = __builtin_amdgcn_permlane16_swap(__float_as_uint(o0[e]), __float_as_uint(o1[e]), false, false);
          const unsigned xa = xv[i][e >> 1], ba = bv[i][e >> 1], xb = xv[i][2 + (e >> 1)], bb = bv[i][2 + (e >> 1)];
          base[i][e] = __uint_as_float(r[0]) + ((e & 1) ? bf_hi(xa) + bf_hi(ba) : bf_lo(xa) + bf_lo(ba));
          base[i][4 + e] = __uint_as_float(r[1]) + ((e & 1) ? bf_hi(xb) + bf_hi(bb) : bf_lo(xb) + bf_lo(bb));
        }
      }
      SBAR();
#pragma unroll 1
      for (int it = 0; it < (1 << 14); ++it) {
        unsigned mx = 0u;
#pragma unroll
        for (int q = 0; q < 3; ++q)
#pragma unroll
          for (int i = 0; i < 3; ++i)
#pragma unroll
            for (int e = 0; e < 4; ++e) mx = mx > t[q][i][e] ? mx : t[q][i][e];
        if (!__any(mx == ~0u)) break;
        asm volatile("" ::: "memory");
        pload();
      }
#pragma unroll
      for (int i = 0; i < 3; ++i) {
        u32x4 o;
#pragma unroll
        for (int e = 0; e < 4; ++e)
          o[e] = cvtpk(((base[i][2 * e] + bf_lo(t[0][i][e])) + bf_lo(t[1][i][e])) + bf_lo(t[2][i][e]), ((base[i][2 * e + 1] + bf_hi(t[0][i][e])) + bf_hi(t[1][i][e])) + bf_hi(t[2][i][e]));
        if (i < np) *(u32x4*)(out + rowoff + 16 * (p0 + i) + 8 * h) = o;
      }
    }
  }
}

extern "C" void kernel_launch(void* const* d_in, const int* in_sizes, int n_in, void* d_out, int out_size, void* d_ws, size_t ws_size, hipStream_t stream) {
  const u16* x = (const u16*)d_in[0];
  const u16* wq = (const u16*)d_in[1];
  const u16* wk = (const u16*)d_in[2];
  const u16* wv = (const u16*)d_in[3];
  const u16* wo = (const u16*)d_in[4];
  const u16* w1 = (const u16*)d_in[6];
  const u16* b1 = (const u16*)d_in[7];
  const u16* w2 = (const u16*)d_in[8];
  const u16* b2 = (const u16*)d_in[9];
  char* ws = (char*)d_ws;
  (void)hipFuncSetAttribute((const void*)k_main, hipFuncAttributeMaxDynamicSharedMemorySize, L_TOTAL);
  k_main<<<256, 512, L_TOTAL, stream>>>(x, wq, wk, wv, wo, w1, b1, w2, b2, ws, (u16*)d_out);
}
```
